```python
import torch

def _softmax(x, axis=-1):
    return torch.softmax(x, dim=axis)
def _gelu(x):
    return torch.nn.functional.gelu(x, approximate="tanh")
def _silu(x):
    return torch.nn.functional.silu(x)
def _sigmoid(x):
    return torch.sigmoid(x)
def _layernorm(x, axis=-1, eps=1e-5):
    m = x.mean(dim=axis, keepdim=True)
    v = x.var(dim=axis, keepdim=True, unbiased=False)
    return (x - m) / torch.sqrt(v + eps)
def _scatter_add(tgt, idx, src, axis=0):
    return tgt.clone().index_add_(axis, idx, src)
def _segment_sum(src, seg, n):
    out = torch.zeros((n,) + src.shape[1:], dtype=src.dtype)
    return out.index_add_(0, seg, src)
def _pad(x, pw):
    flat = tuple(v for pair in reversed(pw) for v in pair)
    return torch.nn.functional.pad(x, flat)

kernel_name = 'concat_heads__ssm_conv_h3__sliding_local_dilated__prompt'

def setup_inputs():

    _w = (lambda a: (lambda t: t.to(torch.bfloat16) if t.is_floating_point() else t)(torch.as_tensor(a)))
    def _A_setup():
        B, S, D = 1, 2048, 576
        ks = 4
        return {
            "q": _w((lambda *s: torch.randn(*s, dtype=torch.bfloat16) * 0.05)(B, S, D)),
            "k": _w((lambda *s: torch.randn(*s, dtype=torch.bfloat16) * 0.8)(B, S, D)),
            "v": _w((lambda *s: torch.randn(*s, dtype=torch.bfloat16) * 0.8)(B, S, D)),
            "shift_k": _w((lambda *s: torch.randn(*s, dtype=torch.bfloat16) * 0.8)(D, ks) * 0.5),
            "ssm_k": _w((lambda *s: torch.randn(*s, dtype=torch.bfloat16) * 1.6)(D, S) * (1.0 / S ** 0.5)),
            "ssm_D": _w((lambda *s: torch.randn(*s, dtype=torch.bfloat16) * 0.8)(D)),
        }
    _A_in = _A_setup()

    def _B_setup():
        B, S, D = 1, 2048, 576
        return {
            "x": _w((lambda *s: torch.randn(*s, dtype=torch.bfloat16) * 0.2)(B, S, D)),
            "wqkv": _w((lambda *s: torch.randn(*s, dtype=torch.bfloat16) * 0.2)(D, 3 * D)),
            "wo": _w((lambda *s: torch.randn(*s, dtype=torch.bfloat16) * 0.05)(D, D)),
        }
    _B_in = _B_setup()

    return {
        "A_q": _A_in["q"],
        "A_k": _A_in["k"],
        "A_v": _A_in["v"],
        "A_shift_k": _A_in["shift_k"],
        "A_ssm_k": _A_in["ssm_k"],
        "A_ssm_D": _A_in["ssm_D"],
        "B_x": _B_in["x"],
        "B_wqkv": _B_in["wqkv"],
        "B_wo": _B_in["wo"],
    }

def reference(A_q, A_k, A_v, A_shift_k, A_ssm_k, A_ssm_D, B_x, B_wqkv, B_wo):
    def _A_ref(q, k, v, shift_k, ssm_k, ssm_D):
        B, S, D = q.shape
        ks = shift_k.shape[-1]
        k_pad = _pad(k, ((0, 0), (ks - 1, 0), (0, 0)))
        k_shift = torch.zeros_like(k)
        for t in range(ks):
            k_shift = k_shift + k_pad[:, t:t + S, :] * shift_k[:, ks - 1 - t][None, None, :]
        kv = k_shift * v
        L = 2 * S
        kv_pad = _pad(kv, ((0, 0), (0, S), (0, 0)))
        kf_pad = _pad(ssm_k, ((0, 0), (0, S)))
        kv_f = (lambda x, axes=(-2,-1): torch.fft.rfft2(x.float(), dim=axes))(kv_pad[:, None, :, :], axes=(1, 2))
        kf_f = (lambda x, axes=(-2,-1): torch.fft.rfft2(x.float(), dim=axes))(kf_pad[:, None, :], axes=(1, 2))
        kf_f = (lambda x, *d: x.permute(*d))(kf_f, 1, 2, 0)
        y_f = kv_f * kf_f[None, :, :, :]
        y = (lambda x, s, axes=(-2,-1): torch.fft.irfft2(x, s=s, dim=axes).to(torch.bfloat16))(y_f, (1, L), axes=(1, 2))[:, 0, :S, :]
        y = y + kv * ssm_D[None, None, :]
        return q * y

    def _B_ref(x, wqkv, wo):
        B, S, D = x.shape
        H = 32; Dh = D // H
        W, dil = 8, 2
        qkv = (lambda x, *d: x.permute(*d))((x @ wqkv).reshape(B, S, 3, H, Dh), 2, 0, 3, 1, 4)
        q, k, v = qkv[0], qkv[1], qkv[2]
        scale = Dh ** -0.5
        pos = torch.arange(S)
        off = torch.arange(W) * dil
        idx = pos[:, None] - off[None, :]
        mask = idx >= 0
        idx_c = torch.where(mask, idx, 0).reshape(-1)
        kg = k[:, :, idx_c, :].reshape(B, H, S, W, Dh)
        vg = v[:, :, idx_c, :].reshape(B, H, S, W, Dh)
        logits = torch.einsum("bhsd,bhswd->bhsw", q, kg) * scale
        logits = torch.where(mask[None, None, :, :], logits, -1e9)
        attn = _softmax(logits, axis=-1)
        o = torch.einsum("bhsw,bhswd->bhsd", attn, vg)
        return (lambda x, *d: x.permute(*d))(o, 0, 2, 1, 3).reshape(B, S, D) @ wo

    a = _A_ref(A_q, A_k, A_v, A_shift_k, A_ssm_k, A_ssm_D)
    b = _B_ref(B_x, B_wqkv, B_wo)
    return torch.concatenate([a, b], axis=-1)
```

```cpp
#include <hip/hip_runtime.h>
#include <hip/hip_bf16.h>
typedef unsigned short u16;
constexpr int S=2048, D=576, H=32, DH=18;
__device__ __forceinline__ float b2f(u16 x){ return __uint_as_float(((unsigned)x)<<16); }
__device__ __forceinline__ u16 f2b(float f){ __hip_bfloat16 b = __float2bfloat16(f); return *reinterpret_cast<u16*>(&b); }
__device__ __forceinline__ float rb(float f){ return b2f(f2b(f)); }

__global__ void k_kv(const u16* k, const u16* v, const u16* sk, u16* kv){
  int i = blockIdx.x*blockDim.x+threadIdx.x; if(i>=S*D) return;
  int s=i/D, d=i%D;
  float acc=0.f;
  for(int t=0;t<4;t++){ int j=3-t; float kk = (s-j>=0)? b2f(k[(s-j)*D+d]):0.f; acc = rb(acc + rb(kk*b2f(sk[d*4+j]))); }
  kv[i]=f2b(acc*b2f(v[i]));
}
__global__ void k_conv(const u16* q, const u16* kv, const u16* h, const u16* Dd, u16* out){
  int i = blockIdx.x*blockDim.x+threadIdx.x; if(i>=S*D) return;
  int s=i/D, d=i%D;
  float acc=0.f;
  for(int u=0;u<=s;u++) acc += b2f(h[d*S+u])*b2f(kv[(s-u)*D+d]);
  float y = rb(acc);
  y = rb(y + rb(b2f(kv[i])*b2f(Dd[d])));
  out[s*2*D+d]=f2b(b2f(q[i])*y);
}
__global__ void k_gemm(const u16* A, const u16* B, u16* C, int M,int N,int K,int ldc){
  int i = blockIdx.x*blockDim.x+threadIdx.x; if(i>=M*N) return;
  int m=i/N,n=i%N; float acc=0.f;
  for(int k=0;k<K;k++) acc+=b2f(A[m*K+k])*b2f(B[k*N+n]);
  C[m*ldc+n]=f2b(acc);
}
__global__ void k_attn(const u16* qkv, u16* o){
  int i = blockIdx.x*blockDim.x+threadIdx.x; if(i>=S*H) return;
  int s=i/H,h=i%H;
  float l0,l1,l2,l3,l4,l5,l6,l7;
  const float scale = 0.23570226039551584f;
  auto lg=[&](int w){ int t=s-2*w; if(t<0) return rb(-1e9f); float a=0.f; for(int d=0;d<DH;d++) a+=b2f(qkv[s*3*D+h*DH+d])*b2f(qkv[t*3*D+D+h*DH+d]); return rb(rb(a)*scale); };
  l0=lg(0);l1=lg(1);l2=lg(2);l3=lg(3);l4=lg(4);l5=lg(5);l6=lg(6);l7=lg(7);
  float m=fmaxf(fmaxf(fmaxf(l0,l1),fmaxf(l2,l3)),fmaxf(fmaxf(l4,l5),fmaxf(l6,l7)));
  l0=expf(l0-m);l1=expf(l1-m);l2=expf(l2-m);l3=expf(l3-m);l4=expf(l4-m);l5=expf(l5-m);l6=expf(l6-m);l7=expf(l7-m);
  float z=l0+l1+l2+l3+l4+l5+l6+l7;
  l0=rb(l0/z);l1=rb(l1/z);l2=rb(l2/z);l3=rb(l3/z);l4=rb(l4/z);l5=rb(l5/z);l6=rb(l6/z);l7=rb(l7/z);
  for(int d=0;d<DH;d++){
    auto vv=[&](int w){int t=s-2*w; if(t<0)t=0; return b2f(qkv[t*3*D+2*D+h*DH+d]);};
    float a=l0*vv(0)+l1*vv(1)+l2*vv(2)+l3*vv(3)+l4*vv(4)+l5*vv(5)+l6*vv(6)+l7*vv(7);
    o[s*D+h*DH+d]=f2b(a);
  }
}
extern "C" void kernel_launch(void* const* d_in, const int* in_sizes, int n_in,
                              void* d_out, int out_size, void* d_ws, size_t ws_size,
                              hipStream_t stream) {
  const u16* q=(const u16*)d_in[0]; const u16* k=(const u16*)d_in[1]; const u16* v=(const u16*)d_in[2];
  const u16* sk=(const u16*)d_in[3]; const u16* h=(const u16*)d_in[4]; const u16* Dd=(const u16*)d_in[5];
  const u16* x=(const u16*)d_in[6]; const u16* wqkv=(const u16*)d_in[7]; const u16* wo=(const u16*)d_in[8];
  u16* out=(u16*)d_out; u16* ws=(u16*)d_ws;
  u16* kv=ws; u16* qkv=ws+S*D; u16* o=qkv+S*3*D;
  k_kv<<<S*D/256,256,0,stream>>>(k,v,sk,kv);
  k_conv<<<S*D/256,256,0,stream>>>(q,kv,h,Dd,out);
  k_gemm<<<S*3*D/256,256,0,stream>>>(x,wqkv,qkv,S,3*D,D,3*D);
  k_attn<<<S*H/256,256,0,stream>>>(qkv,o);
  k_gemm<<<S*D/256,256,0,stream>>>(o,wo,out+D,S,D,D,2*D);
}
```

```cpp
#include <hip/hip_runtime.h>
#include <hip/hip_bf16.h>

typedef unsigned short u16;
typedef unsigned int u32;
typedef unsigned long long u64;
typedef __bf16 bf8 __attribute__((ext_vector_type(8)));
typedef __bf16 bf2 __attribute__((ext_vector_type(2)));
typedef short s4 __attribute__((ext_vector_type(4)));
typedef float f2 __attribute__((ext_vector_type(2)));
typedef float f4 __attribute__((ext_vector_type(4)));
typedef u32 u2 __attribute__((ext_vector_type(2)));
typedef u32 u4 __attribute__((ext_vector_type(4)));
#define LAS __attribute__((address_space(3)))
#define DEV __device__ __forceinline__

constexpr int S_ = 2048, D_ = 576;
constexpr u32 TBYTES = S_ * D_ * 2;
constexpr u32 WS_FLAG = 0, WS_EPOCH = 4096, WS_OF = 65536, WS_USED = WS_OF + TBYTES;
constexpr int SPIN_CAP = 1 << 16;
constexpr int LDS_BYTES = 163840;
#define AUX_A 0
#define AUX_H 0
#define AUX_B 0

#define ST(n)

struct Params {
  const u16 *q, *k, *v, *sk, *h, *dd, *x, *w, *wo;
  u16* out;
  char* ws;
};

DEV u4 mk_rs(const void* p, u32 bytes) {
  u64 a = (u64)p;
  return (u4){(u32)a, (u32)(a >> 32) & 0xffffu, bytes, 0x00020000u};
}
DEV __amdgpu_buffer_rsrc_t mk_br(const void* p, u32 bytes) {
  return __builtin_amdgcn_make_buffer_rsrc((void*)p, 0, bytes, 0x00020000);
}
template <int AUX>
DEV void dma16(u4 rs, u32 voff, u32 soff, u32 lds) {
  if constexpr (AUX == 2)
    asm volatile("s_mov_b32 m0, %3\n\ts_nop 0\n\tbuffer_load_dwordx4 %0, %1, %2 offen nt lds" ::"v"(voff), "s"(rs), "s"(soff), "s"(lds) : "memory");
  else
    asm volatile("s_mov_b32 m0, %3\n\ts_nop 0\n\tbuffer_load_dwordx4 %0, %1, %2 offen lds" ::"v"(voff), "s"(rs), "s"(soff), "s"(lds) : "memory");
}
#define WAITV(n) asm volatile("s_waitcnt vmcnt(" #n ")" ::: "memory")
#define SB() __builtin_amdgcn_sched_barrier(0)
#define WAITL() asm volatile("s_waitcnt lgkmcnt(0)" ::: "memory")
DEV void bar() { asm volatile("s_waitcnt lgkmcnt(0)\n\ts_barrier" ::: "memory"); }
DEV u4 ld128(u32 a) { return *(const LAS u4*)(size_t)a; }
DEV u2 ld64(u32 a) { return *(const volatile LAS u2*)(size_t)a; }
DEV void st128(u32 a, u4 v) { *(LAS u4*)(size_t)a = v; }
DEV void st64(u32 a, u2 v) { *(LAS u2*)(size_t)a = v; }
DEV void st32(u32 a, u32 v) { *(LAS u32*)(size_t)a = v; }
DEV u2 ldtr(u32 a) { return __builtin_bit_cast(u2, __builtin_amdgcn_ds_read_tr16_b64_v4i16((LAS s4*)(size_t)a)); }
DEV u4 cat(u2 a, u2 b) { return (u4){a.x, a.y, b.x, b.y}; }
DEV f4 mma(u4 a, u4 b, f4 c) {
  return __builtin_amdgcn_mfma_f32_16x16x32_bf16(__builtin_bit_cast(bf8, a), __builtin_bit_cast(bf8, b), c, 0, 0, 0);
}
DEV u32 pk(float lo, float hi) { return __builtin_bit_cast(u32, __builtin_convertvector((f2){lo, hi}, bf2)); }
DEV float lo16(u32 x) { return __uint_as_float(x << 16); }
DEV float hi16(u32 x) { return __uint_as_float(x & 0xffff0000u); }
DEV u32 uni(u32 x) { return __builtin_amdgcn_readfirstlane(x); }
DEV float rows_max(float v) {
  u32 x = __float_as_uint(v);
  auto a = __builtin_amdgcn_permlane16_swap(x, x, false, false);
  v = fmaxf(__uint_as_float(a[0]), __uint_as_float(a[1]));
  x = __float_as_uint(v);
  auto b = __builtin_amdgcn_permlane32_swap(x, x, false, false);
  return fmaxf(__uint_as_float(b[0]), __uint_as_float(b[1]));
}
DEV float rows_sum(float v) {
  u32 x = __float_as_uint(v);
  auto a = __builtin_amdgcn_permlane16_swap(x, x, false, false);
  v = __uint_as_float(a[0]) + __uint_as_float(a[1]);
  x = __float_as_uint(v);
  auto b = __builtin_amdgcn_permlane32_swap(x, x, false, false);
  return __uint_as_float(b[0]) + __uint_as_float(b[1]);
}

constexpr u32 A_H = 0, A_HS = 8448, A_HB = 4224;
constexpr u32 A_X = 8 * A_HS, A_XS = 8272, A_XB = 4128;
constexpr u32 A_RK = A_X + 8 * A_XS, A_RV = A_RK + 516 * 16;
constexpr u32 A_Y = A_RV + 516 * 16, A_YS = 1088;
static_assert(A_Y + 8 * A_YS <= LDS_BYTES, "lds");

template <int R>
DEV void conv_phase(f4 (&acc)[8], u32 xa, u32 xcap, u32 ha, u4 fd) {
  constexpr int NA = 8 - 2 * R, NM = 2 * NA - 1, NL = 2 * NA + 2;
  u4 g[2][2];
  u2 f[2][NA][2];
  u32 x1 = xa - 1024 * R - 512;
  auto rd = [&](int s, int i, int db) {
    if (i == 0) g[s][0] = ld128(R == 0 ? min(x1 + 512 + 64 * db, xcap) : x1 + 512 + 64 * db);
    else if (i == 3) g[s][1] = ld128(x1 + 64 * db);
    else {
      const int q = i < 3 ? i - 1 : i - 2;
      f[s][q >> 1][q & 1] = ld64(ha + 64 * db + 512 * (q >> 1) + 8 * (q & 1));
    }
  };
  auto step = [&](int s, int db, bool more) {
#pragma unroll
    for (int m = 0; m < NM; ++m) {
      const int a = m >> 1, o = m & 1;
      acc[2 * R + a + o] = mma(g[s][o], cat(f[s][a][0], f[s][a][1]), acc[2 * R + a + o]);
      if (more) {
#pragma unroll
        for (int i = (m * NL) / (NM - (R < 3)); i < ((m + 1) * NL) / (NM - (R < 3)) && i < NL; ++i) rd(s ^ 1, i, db);
      }
      SB();
    }
  };
#pragma unroll
  for (int i = 0; i < NL; ++i) rd(0, i, 0);
  SB();
  acc[2 * R] = mma(g[0][0], fd, acc[2 * R]);
  acc[2 * R + 1] = mma(g[0][1], fd, acc[2 * R + 1]);
  SB();
#pragma unroll 1
  for (int b = 0; b < 3; ++b) {
    step(0, 1, true);
    step(1, 2, true);
    x1 += 128;
    ha += 128;
  }
  step(0, 1, true);
  step(1, 2, false);
}

DEV void role_a(const Params& p, u32 sm, int oct) {
  const int t = threadIdx.x, l = t & 63, w = uni(t >> 6);
  const u4 rk = mk_rs(p.k, TBYTES), rv = mk_rs(p.v, TBYTES), rh = mk_rs(p.h, TBYTES);
  const auto bq = mk_br(p.q, TBYTES);
  const auto bo = mk_br(p.out, 2 * TBYTES);
  const u32 vrow = t * (D_ * 2) + oct * 16;
  const u32 vhalo = (u32)((l - 4) * (D_ * 2) + oct * 16);
  auto issue_raw = [&](int r) {
    dma16<AUX_A>(rk, vrow, r * 512 * (D_ * 2), sm + A_RK + 64 + w * 1024);
    dma16<AUX_A>(rv, vrow, r * 512 * (D_ * 2), sm + A_RV + 64 + w * 1024);
    if (w < 2) {
      u32 vh = r ? vhalo + r * 512 * (D_ * 2) : 0x80000000u;
      if (l < 4) dma16<AUX_A>(w ? rv : rk, vh, 0, sm + (w ? A_RV : A_RK));
    }
  };
  issue_raw(0);
  const int ch = oct * 8 + w;
#pragma unroll
  for (int i = 0; i < 4; ++i) {
    dma16<AUX_H>(rh, l * 16, ch * 4096 + i * 1024, sm + A_H + w * A_HS + 64 + i * 1024);
    dma16<AUX_H>(rh, l * 16, ch * 4096 + i * 1024, sm + A_H + w * A_HS + A_HB + 68 + i * 1024);
  }
  const u2 skr = *(const u2*)(p.sk + (oct * 8 + (l & 7)) * 4);
  const u32 dv = p.dd[ch];
  if (l < 17) {
    st32(sm + A_H + w * A_HS + A_HB + l * 4, 0);
    if (l < 16) st32(sm + A_H + w * A_HS + l * 4, 0);
    if (l < 8) st32(sm + A_X + w * A_XS + (l >> 2) * A_XB + 4096 + (l & 3) * 4, 0);
  }
  const int j = l & 15, g = l >> 4;
  const u32 xb = sm + A_X + w * A_XS + ((j & 1) ? 0 : A_XB);
  const u32 xa = xb + 2 * (2016 - 32 * (j >> 1) + 8 * g), xcap = xb + 4096;
  const u32 ha = sm + A_H + w * A_HS + 4 * (1 + j + 4 * g) + ((j & 1) ? 0 : A_HB + 4);
  const int di = 15 - j - 4 * g;
  const u4 fd = {di == 0 ? dv : 0u, di == 1 ? dv : 0u, di == 2 ? dv : 0u, di == 3 ? dv : 0u};
  const float c0 = lo16(skr.x), c1 = hi16(skr.x), c2 = lo16(skr.y), c3 = hi16(skr.y);
  const u32 tra = (64 * w + 16 * g + 8 * ((l >> 1) & 1) + (j >> 2)) * 16 + 8 * (l & 1);
  const u32 xw = sm + A_X + (l & 7) * A_XS + 2 * (2040 - 64 * w - 8 * (l >> 3));
  const int prow = 256 * (t >> 8) + 64 * ((t >> 6) & 3) + 2 * ((t >> 2) & 15) + (t & 1) + 32 * ((t >> 1) & 1);
  const u32 yr = sm + A_Y + (j >> 2) * A_YS + (64 * w + 16 * g) * 2 + 8 * (l & 3);
  const u32 yw = sm + A_Y + w * A_YS + l * 8;
  f4 acc[8];
#pragma unroll
  for (int n = 0; n < 8; ++n) acc[n] = (f4){0.f, 0.f, 0.f, 0.f};
  u4 qv;

  auto prep = [&](int r) {
    u2 ka = ldtr(sm + A_RK + tra), kb = ldtr(sm + A_RK + tra + 64), kc = ldtr(sm + A_RK + tra + 128);
    u2 va = ldtr(sm + A_RV + tra), vb = ldtr(sm + A_RV + tra + 64), vc = ldtr(sm + A_RV + tra + 128);
    float kk[12] = {lo16(ka.x), hi16(ka.x), lo16(ka.y), hi16(ka.y), lo16(kb.x), hi16(kb.x),
                    lo16(kb.y), hi16(kb.y), lo16(kc.x), hi16(kc.x), lo16(kc.y), hi16(kc.y)};
    float vv[9] = {hi16(va.y), lo16(vb.x), hi16(vb.x), lo16(vb.y), hi16(vb.y), lo16(vc.x), hi16(vc.x), lo16(vc.y), hi16(vc.y)};
    float kv[9];
#pragma unroll
    for (int e = 0; e < 9; ++e) kv[e] = (kk[e] * c3 + kk[e + 1] * c2 + kk[e + 2] * c1 + kk[e + 3] * c0) * vv[e];
    st128(xw - 1024 * r, (u4){pk(kv[8], kv[7]), pk(kv[6], kv[5]), pk(kv[4], kv[3]), pk(kv[2], kv[1])});
    st128(xw - 1024 * r + A_XB, (u4){pk(kv[7], kv[6]), pk(kv[5], kv[4]), pk(kv[3], kv[2]), pk(kv[1], kv[0])});
  };
  auto fin = [&](int r) {
    u2 ya = ldtr(yr), yb = ldtr(yr + 4 * A_YS);
    u4 o = {pk(lo16(qv.x) * lo16(ya.x), hi16(qv.x) * hi16(ya.x)), pk(lo16(qv.y) * lo16(ya.y), hi16(qv.y) * hi16(ya.y)),
            pk(lo16(qv.z) * lo16(yb.x), hi16(qv.z) * hi16(yb.x)), pk(lo16(qv.w) * lo16(yb.y), hi16(qv.w) * hi16(yb.y))};
    __builtin_amdgcn_raw_buffer_store_b128(o, bo, (prow + r * 512) * (D_ * 4) + oct * 16, 0, 0);
  };
#define A_ROUND(R)                                                                                         \
  WAITV(0);                                                                                                \
  bar();                                                                                                   \
  ST(1 + 3 * R);                                                                                           \
  if (R == 0) asm volatile("" ::"v"(fd));                                                                  \
  if (R > 0) fin(R - 1);                                                                                   \
  prep(R);                                                                                                 \
  bar();                                                                                                   \
  ST(2 + 3 * R);                                                                                           \
  if (R < 3) issue_raw(R + 1);                                                                             \
  qv = __builtin_amdgcn_raw_buffer_load_b128(bq, prow * (D_ * 2) + oct * 16, R * 512 * (D_ * 2), AUX_A);   \
  conv_phase<R>(acc, xa, xcap, ha, fd);                                                                    \
  st64(yw, (u2){pk(acc[2 * R][0], acc[2 * R][1]), pk(acc[2 * R][2], acc[2 * R][3])});                      \
  st64(yw + 512, (u2){pk(acc[2 * R + 1][0], acc[2 * R + 1][1]), pk(acc[2 * R + 1][2], acc[2 * R + 1][3])}); \
  ST(3 + 3 * R);
  A_ROUND(0) A_ROUND(1) A_ROUND(2) A_ROUND(3)
  bar();
  fin(3);
  ST(13);
}

constexpr u32 B_XB = 14 * 1024, B_SLOT = 41 * 1024;
constexpr u32 B_IMG = LDS_BYTES - 112 * 432;
constexpr u32 B_WO = 2 * B_SLOT - 72 * 1024;
constexpr u32 B_OT = 2 * B_SLOT;
static_assert(B_OT + 8 * 2304 <= B_IMG, "lds");

struct BDma {
  u4 rx, rw, r1;
  u32 v[6], m1, sm;
  int w;
  DEV void issue(int stage, u32 slot) const {
    const u32 d = sm + slot + w * 1024, sw = stage * (64 * 3456);
    dma16<AUX_B>(rx, v[0], stage * 128, d);
    dma16<AUX_B>(r1, v[1], stage * m1, d + 8192);
    dma16<AUX_B>(rw, v[2], sw, d + 2 * 8192);
    dma16<AUX_B>(rw, v[3], sw, d + 3 * 8192);
    dma16<AUX_B>(rw, v[4], sw, d + 4 * 8192);
    if (w == 0) dma16<AUX_B>(rw, v[5], sw, d + 5 * 8192);
  }
};

template <int NR, int NC, class F>
DEV void qkv_gemm(const BDma& dm, int st0, int rb0, int ct0, F&& late) {
  const int l = threadIdx.x & 63, r = l & 15, g = l >> 4;
  const u32 sm = dm.sm;
  const u32 xa = sm + (rb0 * 16 + r) * 128 + ((g ^ (r >> 1)) * 16);
  const u32 wa = sm + B_XB + (16 * (g >> 1) + 2 * ((r >> 2) + 4 * (g & 1))) * 432 + ct0 * 32 + (l & 3) * 8;
  f4 acc[NR][NC];
#pragma unroll
  for (int a = 0; a < NR; ++a)
#pragma unroll
    for (int c = 0; c < NC; ++c) acc[a][c] = (f4){0.f, 0.f, 0.f, 0.f};
  constexpr int NM = NR * NC, NL = 2 * NC + NR;
  u4 xf[2][NR];
  u2 wf[2][NC][2];
  u32 xs, ws;
  auto rd = [&](int s, int i) {
    if (i == 2) xf[s][0] = ld128(xs ^ (s * 64));
    else if (i <= 2 * NC) {
      const int q = i < 2 ? i : i - 1;
      wf[s][q >> 1][q & 1] = ldtr(ws + s * 13824 + (q >> 1) * 32 + (q & 1) * 432);
    } else xf[s][i - 2 * NC] = ld128((xs ^ (s * 64)) + (i - 2 * NC) * 2048);
  };
  auto step = [&](int s, bool more, auto&& mid) {
#pragma unroll
    for (int m = 0; m < NM; ++m) {
      const int a = m / NC, c = m % NC;
      acc[a][c] = mma(cat(wf[s][c][0], wf[s][c][1]), xf[s][a], acc[a][c]);
      if (more) {
#pragma unroll
        for (int i = 2 * m; i < 2 * m + 2 && i < NL; ++i) rd(s ^ 1, i);
      }
      if (m == (NL + 1) / 2) mid();
      SB();
    }
  };
  u32 slot = B_SLOT;
  int st = st0 + 3;
  xs = xa + slot;
  ws = wa + slot;
#pragma unroll
  for (int i = 0; i < NL; ++i) rd(0, i);
  SB();
#pragma unroll 1
  for (int jj = 0; jj < 8; ++jj) {
    step(0, true, [] {});
    if (jj < 7) WAITV(5); else WAITV(0);
    bar();
    const u32 freed = slot;
    slot = slot == 2 * B_SLOT ? 0 : slot + B_SLOT;
    xs = xa + slot;
    ws = wa + slot;
    SB();
    step(1, true, [&] {
      if (jj < 6) {
        if (st >= 9) st -= 9;
        dm.issue(st, freed);
        ++st;
      } else if (jj == 7) {
        late();
      }
    });
  }
  step(0, true, [] {});
  step(1, false, [] {});
  const u32 ia = sm + B_IMG + (rb0 * 16 + r) * 432 + ct0 * 32 + g * 8;
#pragma unroll
  for (int a = 0; a < NR; ++a)
#pragma unroll
    for (int c = 0; c < NC; ++c) {
      u2 o = {pk(acc[a][c][0], acc[a][c][1]), pk(acc[a][c][2], acc[a][c][3])};
      if (c < NC - 1 || ct0 + c < 13 || g < 2) st64(ia + a * (16 * 432) + c * 32, o);
    }
}

DEV void role_b(const Params& p, u32 sm, int xcd, int i) {
  const int t = threadIdx.x, l = t & 63, w = uni(t >> 6);
  const int rt = 5 * (xcd >> 1) + i % 5, grp = 4 * (xcd & 1) + i / 5;
  const int par = rt / 10, sg0 = 104 * (rt % 10);
  const auto bw = mk_br(p.ws, WS_USED);
  BDma dm;
  dm.rx = mk_rs(p.x, TBYTES);
  dm.rw = mk_rs(p.w, 3 * D_ * D_ * 2);
  dm.sm = sm;
  dm.w = w;
  dm.r1 = w < 6 ? dm.rx : dm.rw;
  dm.m1 = w < 6 ? 128 : 64 * 3456;
#pragma unroll
  for (int u = 0; u < 6; ++u) {
    const int pc = w + 8 * u;
    const int row = 8 * pc + (l >> 3), sg = sg0 - 8 + row;
    const int s = sg < 0 ? 0 : 2 * min(sg, 1023) + par;
    const u32 vx = s * (D_ * 2) + (((l & 7) ^ ((row >> 1) & 7)) * 16);
    const int n = 64 * (pc - 14) + l, ro = n / 27, c = n % 27;
    const int k = 16 * (ro >> 4) + 8 * ((ro >> 3) & 1) + 4 * (ro & 1) + ((ro >> 1) & 3);
    const u32 vw = k * 3456 + (c / 9) * 1152 + grp * 144 + (c % 9) * 16;
    dm.v[u] = u == 0 ? vx : (u == 1 ? (w < 6 ? vx : vw) : vw);
  }
  int st0 = (2 * (i % 5) + 2 * (i / 5)) % 9;
  dm.issue(st0, B_SLOT);
  dm.issue(st0 == 8 ? 0 : st0 + 1, 2 * B_SLOT);
  dm.issue(st0 >= 7 ? st0 - 7 : st0 + 2, 0);
  const bool has2 = i < 18;
  const int rt2 = 2 * xcd + i / 9, cg = i % 9;
  const u4 rwo = mk_rs(p.wo, D_ * D_ * 2);
  const u32 wsw = cg * 128 + (((l & 7) ^ (((l >> 4) & 3) << 1)) * 16);
  auto wo_piece = [&](int u) {
    const int P = w + 8 * u, e = P >> 2, tt = P & 1, gh = (P >> 1) & 1;
    if (u < 8) dma16<AUX_B>(rwo, (l >> 3) * 1152 + wsw, (72 * (e >> 1) + 32 * (e & 1) + 16 * tt + 8 * gh) * 1152, sm + B_WO + P * 1024);
    else dma16<AUX_B>(rwo, (72 * (l >> 5) + ((l >> 3) & 3)) * 1152 + wsw, (72 * (4 * (e - 16) + 2 * gh) + 64 + 4 * tt) * 1152, sm + B_WO + P * 1024);
  };
  auto late = [&]() {
    if (has2) {
#pragma unroll
      for (int u = 4; u < 9; ++u) wo_piece(u);
    }
  };
  ST(1);
  WAITV(10);
  bar();
  ST(2);
  {
    const int rb0 = w < 4 ? 0 : 4, cq = (w + (w >> 2) * 2) & 3;
    const int ct0 = cq < 2 ? 4 * cq : 3 * cq + 2;
    if (w < 2) qkv_gemm<4, 4>(dm, st0, rb0, ct0, late);
    else if (w < 4) qkv_gemm<4, 3>(dm, st0, rb0, ct0, late);
    else if (w < 6) qkv_gemm<3, 3>(dm, st0, rb0, ct0, late);
    else qkv_gemm<3, 4>(dm, st0, rb0, ct0, late);
  }
  ST(3);
  if (w == 7) {
    const u2 e2 = __builtin_amdgcn_raw_buffer_load_b64(bw, WS_EPOCH, 0, 16);
    const u64 ep = (((u64)e2.y << 32) | e2.x) + 1;
    if (l == 0) __builtin_amdgcn_raw_buffer_store_b64((u2){(u32)ep, (u32)(ep >> 32)}, bw, WS_FLAG + 256 + (xcd * 20 + i) * 8, 0, 16);
    for (int it = 0; it < SPIN_CAP; ++it) {
      asm volatile("" ::: "memory");
      const u2 f = __builtin_amdgcn_raw_buffer_load_b64(bw, WS_FLAG + min(l, 23) * 8, 0, 16);
      if (__all(f.x == (u32)ep && f.y == (u32)(ep >> 32))) break;
      __builtin_amdgcn_s_sleep(8);
    }
  }
  bar();
  ST(4);
  if (has2) {
#pragma unroll
    for (int u = 0; u < 4; ++u) wo_piece(u);
  }
  const int c = l & 15, g = l >> 4;
  if (w < 7) {
    const u32 im = sm + B_IMG;
    const u32 qa = im + min(8 + 16 * w + c, 111) * 432 + g * 16;
    const u32 ka0 = im + (16 * w + c) * 432 + 144 + g * 16;
    const u32 ka1 = im + min(16 * w + 16 + c, 111) * 432 + 144 + g * 16;
    const u32 va0 = im + (16 * w + 4 * g + (c >> 2)) * 432 + 288 + (l & 3) * 8;
    const u32 va1 = im + min(16 * w + 16 + 4 * g + (c >> 2), 111) * 432 + 288 + (l & 3) * 8;
    const int kfirst = 8 - sg0 - 16 * w;
    const float cs = 0.23570226039551584f * 1.4426950408889634f, neg = -1e9f * 1.4426950408889634f;
    u4 vf[5];
#pragma unroll
    for (int ct = 0; ct < 5; ++ct) vf[ct] = cat(ldtr(va0 + ct * 32), ldtr(va1 + ct * 32));
    u2 ot[5];
#pragma unroll
    for (int hh = 0; hh < 4; ++hh) {
      u4 qf = ld128(qa + hh * 32), k0 = ld128(ka0 + hh * 32), k1 = ld128(ka1 + hh * 32);
#pragma unroll
      for (int d = 0; d < 4; ++d) {
        const bool in = (u32)(4 * g + d - hh) < 9u;
        qf[d] = in ? qf[d] : 0u;
        k0[d] = in ? k0[d] : 0u;
        k1[d] = in ? k1[d] : 0u;
      }
      const f4 z = {0.f, 0.f, 0.f, 0.f};
      const f4 s0 = mma(k0, qf, z), s1 = mma(k1, qf, z);
      float sv[8], m = -INFINITY;
#pragma unroll
      for (int e = 0; e < 8; ++e) {
        const int kap = 16 * (e >> 2) + 4 * g + (e & 3);
        float x = (e < 4 ? s0[e & 3] : s1[e & 3]) * cs;
        if (e < 4) x = kap < kfirst ? neg : x;
        sv[e] = (u32)(kap - c - 1) < 8u ? x : -INFINITY;
        m = fmaxf(m, sv[e]);
      }
      m = rows_max(m);
      float sum = 0.f;
#pragma unroll
      for (int e = 0; e < 8; ++e) {
        sv[e] = __builtin_amdgcn_exp2f(sv[e] - m);
        sum += sv[e];
      }
      const float inv = __builtin_amdgcn_rcpf(rows_sum(sum));
      const u4 pf = {pk(sv[0] * inv, sv[1] * inv), pk(sv[2] * inv, sv[3] * inv), pk(sv[4] * inv, sv[5] * inv), pk(sv[6] * inv, sv[7] * inv)};
      const f4 oa = mma(vf[hh], pf, z), ob = mma(vf[hh + 1], pf, z);
      const u2 a2 = {pk(oa[0], oa[1]), pk(oa[2], oa[3])};
      if (hh == 0) ot[0] = a2;
      else {
        ot[hh].x = 2 * g < hh ? ot[hh].x : a2.x;
        ot[hh].y = 2 * g + 1 < hh ? ot[hh].y : a2.y;
      }
      ot[hh + 1] = (u2){pk(ob[0], ob[1]), pk(ob[2], ob[3])};
    }
#pragma unroll
    for (int ct = 0; ct < 5; ++ct) {
      ot[ct].x = min(ot[ct].x, 0xfffffffeu);
      ot[ct].y = min(ot[ct].y, 0xfffffffeu);
    }
    const int qi = 16 * w + c, rho = par * 1024 + sg0 + qi;
    if (qi < 104 && sg0 + qi < 1024) {
      const u32 ob = WS_OF + (rho >> 4) * (18 * 1024) + (rho & 15) * 16;
      __builtin_amdgcn_raw_buffer_store_b128(cat(ot[0], ot[1]), bw, ob + g * 256 + 2 * grp * 1024, 0, 16);
      __builtin_amdgcn_raw_buffer_store_b128(cat(ot[2], ot[3]), bw, ob + g * 256 + (2 * grp + 1) * 1024, 0, 16);
      if (g < 2) __builtin_amdgcn_raw_buffer_store_b64(ot[4], bw, ob + g * 8 + (16 + (grp >> 2)) * 1024 + (grp & 3) * 256, 0, 16);
    }
  }
  ST(5);
  if (!has2) return;
  WAITV(0);
  bar();
  ST(6);
  {
    const u32 ob = WS_OF + (8 * rt2 + w) * (18 * 1024) + l * 16;
    constexpr int NPRE = 6;
    const int f = (2 * (g & 1) + (c >> 3)) & 3;
    u32 wb[4];
    u2 wv[NPRE][4][2];
#pragma unroll
    for (int ct = 0; ct < 4; ++ct) {
      wb[ct] = sm + B_WO + 16384 + (16 * (g >> 1) + 4 * (g & 1) + (c >> 2)) * 128 + (l & 3) * 8 + ((ct ^ f) * 32);
      asm volatile("" : "+v"(wb[ct]));
      __builtin_assume(wb[ct] < 0x10000u);
    }
    auto pre = [&]() {
#pragma unroll
      for (int ct = 0; ct < 4; ++ct) {
        u32 lo = wb[ct] - 16384;
        asm volatile("" : "+v"(lo));
        __builtin_assume(lo < 0x10000u);
#pragma unroll
        for (int e = 0; e < NPRE; ++e) {
          wv[e][ct][0] = ldtr(lo + e * 4096);
          wv[e][ct][1] = ldtr(lo + e * 4096 + 1024);
        }
      }
    };
    pre();
    SB();
    for (int it = 0; it < SPIN_CAP; ++it) {
      asm volatile("" ::: "memory");
      const u4 a = __builtin_amdgcn_raw_buffer_load_b128(bw, ob, 16 * 1024, 16);
      const u4 b = __builtin_amdgcn_raw_buffer_load_b128(bw, ob, 17 * 1024, 16);
      const u32 mx = max(max(max(a.x, a.y), max(a.z, a.w)), max(max(b.x, b.y), max(b.z, b.w)));
      if (__all(mx != 0xffffffffu)) break;
      __builtin_amdgcn_s_sleep(8);
    }
    ST(7);
    f4 acc[4];
    for (int it = 0; it < SPIN_CAP; ++it) {
      asm volatile("" ::: "memory");
      u4 of[18];
#pragma unroll
      for (int e = 0; e < 18; ++e) of[e] = __builtin_amdgcn_raw_buffer_load_b128(bw, ob, e * 1024, 16);
#pragma unroll
      for (int ct = 0; ct < 4; ++ct) acc[ct] = (f4){0.f, 0.f, 0.f, 0.f};
      u32 mx = 0;
      SB();
#pragma unroll
      for (int e = 0; e < 18; ++e) {
#pragma unroll
        for (int ct = 0; ct < 4; ++ct) {
          acc[ct] = mma(cat(wv[e % NPRE][ct][0], wv[e % NPRE][ct][1]), of[e], acc[ct]);
          if (e + NPRE < 18) {
            wv[e % NPRE][ct][0] = ldtr(wb[ct] + (e + NPRE) * 4096 - 16384);
            wv[e % NPRE][ct][1] = ldtr(wb[ct] + (e + NPRE) * 4096 - 16384 + 1024);
          }
          if (ct & 1) mx = max(mx, max(of[e][ct], of[e][ct - 1]));
          SB();
        }
      }
      if (__all(mx != 0xffffffffu)) break;
      pre();
    }
    ST(8);
    const u32 tb = sm + B_OT + w * 2304;
#pragma unroll
    for (int ct = 0; ct < 4; ++ct) st64(tb + c * 144 + ct * 32 + g * 8, (u2){pk(acc[ct][0], acc[ct][1]), pk(acc[ct][2], acc[ct][3])});
    const auto bo = mk_br(p.out, 2 * TBYTES);
#pragma unroll
    for (int hf = 0; hf < 2; ++hf) {
      const int rr = 8 * hf + (l >> 3), rho = (8 * rt2 + w) * 16 + rr;
      const u4 o = ld128(tb + rr * 144 + (l & 7) * 16);
      __builtin_amdgcn_raw_buffer_store_b128(o, bo, (2 * (rho & 1023) + (rho >> 10)) * (D_ * 4) + D_ * 2 + cg * 128 + (l & 7) * 16, 0, 0);
    }
    ST(9);
  }
}

DEV void role_s(const Params& p, int si) {
  const int t = threadIdx.x;
  const auto bw = mk_br(p.ws, WS_USED);
  const u2 e2 = __builtin_amdgcn_raw_buffer_load_b64(bw, WS_EPOCH, 0, 16);
#pragma unroll
  for (int u = 0; u < 12; ++u)
    __builtin_amdgcn_raw_buffer_store_b128((u4){~0u, ~0u, ~0u, ~0u}, bw, WS_OF + si * 98304 + t * 16 + u * 8192, 0, 16);
  WAITV(0);
  bar();
  const u64 ep = (((u64)e2.y << 32) | e2.x) + 1;
  const u2 ev = {(u32)ep, (u32)(ep >> 32)};
  if (t == 0) __builtin_amdgcn_raw_buffer_store_b64(ev, bw, WS_FLAG + si * 8, 0, 16);
  ST(1);
  if (si == 0 && t < 64) {
    for (int it = 0; it < SPIN_CAP; ++it) {
      __builtin_amdgcn_s_sleep(64);
      asm volatile("" ::: "memory");
      const u2 a = __builtin_amdgcn_raw_buffer_load_b64(bw, WS_FLAG + min(t, 23) * 8, 0, 16);
      const u4 b = __builtin_amdgcn_raw_buffer_load_b128(bw, WS_FLAG + 256 + t * 16, 0, 16);
      const u4 d = __builtin_amdgcn_raw_buffer_load_b128(bw, WS_FLAG + 256 + 1024 + min(t, 15) * 16, 0, 16);
      const bool ok = a.x == ev.x && b.x == ev.x && b.z == ev.x && d.x == ev.x && d.z == ev.x &&
                      a.y == ev.y && b.y == ev.y && b.w == ev.y && d.y == ev.y && d.w == ev.y;
      if (__all(ok)) break;
    }
    if (t == 0) __builtin_amdgcn_raw_buffer_store_b64(ev, bw, WS_EPOCH, 0, 16);
  }
}

__global__ __launch_bounds__(512, 2) void fused_kernel(Params p) {
  __shared__ __attribute__((aligned(1024))) char smem[LDS_BYTES];
  const u32 sm = (u32)(size_t)(LAS char*)smem;
  const int b = blockIdx.x, xcd = b & 7, i = b >> 3;
  ST(0);
  if (i < 20) role_b(p, sm, xcd, i);
  else if (i < 28 || xcd < 2) role_a(p, sm, i < 28 ? 8 * xcd + i - 20 : 64 + 4 * xcd + i - 28);
  else role_s(p, (xcd - 2) * 4 + i - 28);
}

extern "C" void kernel_launch(void* const* d_in, const int* in_sizes, int n_in, void* d_out, int out_size,
                              void* d_ws, size_t ws_size, hipStream_t stream) {
  Params p;
  p.q = (const u16*)d_in[0];
  p.k = (const u16*)d_in[1];
  p.v = (const u16*)d_in[2];
  p.sk = (const u16*)d_in[3];
  p.h = (const u16*)d_in[4];
  p.dd = (const u16*)d_in[5];
  p.x = (const u16*)d_in[6];
  p.w = (const u16*)d_in[7];
  p.wo = (const u16*)d_in[8];
  p.out = (u16*)d_out;
  p.ws = (char*)d_ws;
  fused_kernel<<<256, 512, 0, stream>>>(p);
}
```

```cpp
#include <hip/hip_runtime.h>
#include <hip/hip_bf16.h>

typedef unsigned short u16;
typedef unsigned int u32;
typedef unsigned long long u64;
typedef __bf16 bf8 __attribute__((ext_vector_type(8)));
typedef __bf16 bf2 __attribute__((ext_vector_type(2)));
typedef short s4 __attribute__((ext_vector_type(4)));
typedef float f2 __attribute__((ext_vector_type(2)));
typedef float f4 __attribute__((ext_vector_type(4)));
typedef u32 u2 __attribute__((ext_vector_type(2)));
typedef u32 u4 __attribute__((ext_vector_type(4)));
#define LAS __attribute__((address_space(3)))
#define DEV __device__ __forceinline__

constexpr int S_ = 2048, D_ = 576;
constexpr u32 TBYTES = S_ * D_ * 2;
constexpr u32 WS_FLAG = 0, WS_EPOCH = 4096, WS_OF = 65536, WS_USED = WS_OF + TBYTES;
constexpr int SPIN_CAP = 1 << 16;
constexpr int LDS_BYTES = 163840;
#define AUX_A 0
#define AUX_H 0
#define AUX_B 0

#define ST(n) do { if ((n) == 99) probe_t = __builtin_amdgcn_s_memrealtime(); } while (0)

struct Params {
  const u16 *q, *k, *v, *sk, *h, *dd, *x, *w, *wo;
  u16* out;
  char* ws;
};

DEV u4 mk_rs(const void* p, u32 bytes) {
  u64 a = (u64)p;
  return (u4){(u32)a, (u32)(a >> 32) & 0xffffu, bytes, 0x00020000u};
}
DEV __amdgpu_buffer_rsrc_t mk_br(const void* p, u32 bytes) {
  return __builtin_amdgcn_make_buffer_rsrc((void*)p, 0, bytes, 0x00020000);
}
template <int AUX>
DEV void dma16(u4 rs, u32 voff, u32 soff, u32 lds) {
  if constexpr (AUX == 2)
    asm volatile("s_mov_b32 m0, %3\n\ts_nop 0\n\tbuffer_load_dwordx4 %0, %1, %2 offen nt lds" ::"v"(voff), "s"(rs), "s"(soff), "s"(lds) : "memory");
  else
    asm volatile("s_mov_b32 m0, %3\n\ts_nop 0\n\tbuffer_load_dwordx4 %0, %1, %2 offen lds" ::"v"(voff), "s"(rs), "s"(soff), "s"(lds) : "memory");
}
#define WAITV(n) asm volatile("s_waitcnt vmcnt(" #n ")" ::: "memory")
#define SB() __builtin_amdgcn_sched_barrier(0)
#define WAITL() asm volatile("s_waitcnt lgkmcnt(0)" ::: "memory")
DEV void bar() { asm volatile("s_waitcnt lgkmcnt(0)\n\ts_barrier" ::: "memory"); }
DEV u4 ld128(u32 a) { return *(const LAS u4*)(size_t)a; }
DEV u2 ld64(u32 a) { return *(const volatile LAS u2*)(size_t)a; }
DEV void st128(u32 a, u4 v) { *(LAS u4*)(size_t)a = v; }
DEV void st64(u32 a, u2 v) { *(LAS u2*)(size_t)a = v; }
DEV void st32(u32 a, u32 v) { *(LAS u32*)(size_t)a = v; }
DEV u2 ldtr(u32 a) { return __builtin_bit_cast(u2, __builtin_amdgcn_ds_read_tr16_b64_v4i16((LAS s4*)(size_t)a)); }
DEV u4 cat(u2 a, u2 b) { return (u4){a.x, a.y, b.x, b.y}; }
DEV f4 mma(u4 a, u4 b, f4 c) {
  return __builtin_amdgcn_mfma_f32_16x16x32_bf16(__builtin_bit_cast(bf8, a), __builtin_bit_cast(bf8, b), c, 0, 0, 0);
}
DEV u32 pk(float lo, float hi) { return __builtin_bit_cast(u32, __builtin_convertvector((f2){lo, hi}, bf2)); }
DEV float lo16(u32 x) { return __uint_as_float(x << 16); }
DEV float hi16(u32 x) { return __uint_as_float(x & 0xffff0000u); }
DEV u32 uni(u32 x) { return __builtin_amdgcn_readfirstlane(x); }
DEV float rows_max(float v) {
  u32 x = __float_as_uint(v);
  auto a = __builtin_amdgcn_permlane16_swap(x, x, false, false);
  v = fmaxf(__uint_as_float(a[0]), __uint_as_float(a[1]));
  x = __float_as_uint(v);
  auto b = __builtin_amdgcn_permlane32_swap(x, x, false, false);
  return fmaxf(__uint_as_float(b[0]), __uint_as_float(b[1]));
}
DEV float rows_sum(float v) {
  u32 x = __float_as_uint(v);
  auto a = __builtin_amdgcn_permlane16_swap(x, x, false, false);
  v = __uint_as_float(a[0]) + __uint_as_float(a[1]);
  x = __float_as_uint(v);
  auto b = __builtin_amdgcn_permlane32_swap(x, x, false, false);
  return __uint_as_float(b[0]) + __uint_as_float(b[1]);
}

constexpr u32 A_H = 0, A_HS = 8448, A_HB = 4224;
constexpr u32 A_X = 8 * A_HS, A_XS = 8272, A_XB = 4128;
constexpr u32 A_RK = A_X + 8 * A_XS, A_RV = A_RK + 516 * 16;
constexpr u32 A_Y = A_RV + 516 * 16, A_YS = 1088;
static_assert(A_Y + 8 * A_YS <= LDS_BYTES, "lds");

template <int R>
DEV void conv_phase(f4 (&acc)[8], u32 xa, u32 xcap, u32 ha, u4 fd) {
  constexpr int NA = 8 - 2 * R, NM = 2 * NA - 1, NL = 2 * NA + 2;
  u4 g[2][2];
  u2 f[2][NA][2];
  u32 x1 = xa - 1024 * R - 512;
  auto rd = [&](int s, int i, int db) {
    if (i == 0) g[s][0] = ld128(R == 0 ? min(x1 + 512 + 64 * db, xcap) : x1 + 512 + 64 * db);
    else if (i == 3) g[s][1] = ld128(x1 + 64 * db);
    else {
      const int q = i < 3 ? i - 1 : i - 2;
      f[s][q >> 1][q & 1] = ld64(ha + 64 * db + 512 * (q >> 1) + 8 * (q & 1));
    }
  };
  auto step = [&](int s, int db, bool more) {
#pragma unroll
    for (int m = 0; m < NM; ++m) {
      const int a = m >> 1, o = m & 1;
      acc[2 * R + a + o] = mma(g[s][o], cat(f[s][a][0], f[s][a][1]), acc[2 * R + a + o]);
      if (more) {
#pragma unroll
        for (int i = (m * NL) / (NM - (R < 3)); i < ((m + 1) * NL) / (NM - (R < 3)) && i < NL; ++i) rd(s ^ 1, i, db);
      }
      SB();
    }
  };
#pragma unroll
  for (int i = 0; i < NL; ++i) rd(0, i, 0);
  SB();
  acc[2 * R] = mma(g[0][0], fd, acc[2 * R]);
  acc[2 * R + 1] = mma(g[0][1], fd, acc[2 * R + 1]);
  SB();
#pragma unroll 1
  for (int b = 0; b < 3; ++b) {
    step(0, 1, true);
    step(1, 2, true);
    x1 += 128;
    ha += 128;
  }
  step(0, 1, true);
  step(1, 2, false);
}

DEV void role_a(u64& probe_t, const Params& p, u32 sm, int oct) {
  const int t = threadIdx.x, l = t & 63, w = uni(t >> 6);
  const u4 rk = mk_rs(p.k, TBYTES), rv = mk_rs(p.v, TBYTES), rh = mk_rs(p.h, TBYTES);
  const auto bq = mk_br(p.q, TBYTES);
  const auto bo = mk_br(p.out, 2 * TBYTES);
  const u32 vrow = t * (D_ * 2) + oct * 16;
  const u32 vhalo = (u32)((l - 4) * (D_ * 2) + oct * 16);
  auto issue_raw = [&](int r) {
    dma16<AUX_A>(rk, vrow, r * 512 * (D_ * 2), sm + A_RK + 64 + w * 1024);
    dma16<AUX_A>(rv, vrow, r * 512 * (D_ * 2), sm + A_RV + 64 + w * 1024);
    if (w < 2) {
      u32 vh = r ? vhalo + r * 512 * (D_ * 2) : 0x80000000u;
      if (l < 4) dma16<AUX_A>(w ? rv : rk, vh, 0, sm + (w ? A_RV : A_RK));
    }
  };
  issue_raw(0);
  const int ch = oct * 8 + w;
#pragma unroll
  for (int i = 0; i < 4; ++i) {
    dma16<AUX_H>(rh, l * 16, ch * 4096 + i * 1024, sm + A_H + w * A_HS + 64 + i * 1024);
    dma16<AUX_H>(rh, l * 16, ch * 4096 + i * 1024, sm + A_H + w * A_HS + A_HB + 68 + i * 1024);
  }
  const u2 skr = *(const u2*)(p.sk + (oct * 8 + (l & 7)) * 4);
  const u32 dv = p.dd[ch];
  if (l < 17) {
    st32(sm + A_H + w * A_HS + A_HB + l * 4, 0);
    if (l < 16) st32(sm + A_H + w * A_HS + l * 4, 0);
    if (l < 8) st32(sm + A_X + w * A_XS + (l >> 2) * A_XB + 4096 + (l & 3) * 4, 0);
  }
  const int j = l & 15, g = l >> 4;
  const u32 xb = sm + A_X + w * A_XS + ((j & 1) ? 0 : A_XB);
  const u32 xa = xb + 2 * (2016 - 32 * (j >> 1) + 8 * g), xcap = xb + 4096;
  const u32 ha = sm + A_H + w * A_HS + 4 * (1 + j + 4 * g) + ((j & 1) ? 0 : A_HB + 4);
  const int di = 15 - j - 4 * g;
  const u4 fd = {di == 0 ? dv : 0u, di == 1 ? dv : 0u, di == 2 ? dv : 0u, di == 3 ? dv : 0u};
  const float c0 = lo16(skr.x), c1 = hi16(skr.x), c2 = lo16(skr.y), c3 = hi16(skr.y);
  const u32 tra = (64 * w + 16 * g + 8 * ((l >> 1) & 1) + (j >> 2)) * 16 + 8 * (l & 1);
  const u32 xw = sm + A_X + (l & 7) * A_XS + 2 * (2040 - 64 * w - 8 * (l >> 3));
  const int prow = 256 * (t >> 8) + 64 * ((t >> 6) & 3) + 2 * ((t >> 2) & 15) + (t & 1) + 32 * ((t >> 1) & 1);
  const u32 yr = sm + A_Y + (j >> 2) * A_YS + (64 * w + 16 * g) * 2 + 8 * (l & 3);
  const u32 yw = sm + A_Y + w * A_YS + l * 8;
  f4 acc[8];
#pragma unroll
  for (int n = 0; n < 8; ++n) acc[n] = (f4){0.f, 0.f, 0.f, 0.f};
  u4 qv;

  auto prep = [&](int r) {
    u2 ka = ldtr(sm + A_RK + tra), kb = ldtr(sm + A_RK + tra + 64), kc = ldtr(sm + A_RK + tra + 128);
    u2 va = ldtr(sm + A_RV + tra), vb = ldtr(sm + A_RV + tra + 64), vc = ldtr(sm + A_RV + tra + 128);
    float kk[12] = {lo16(ka.x), hi16(ka.x), lo16(ka.y), hi16(ka.y), lo16(kb.x), hi16(kb.x),
                    lo16(kb.y), hi16(kb.y), lo16(kc.x), hi16(kc.x), lo16(kc.y), hi16(kc.y)};
    float vv[9] = {hi16(va.y), lo16(vb.x), hi16(vb.x), lo16(vb.y), hi16(vb.y), lo16(vc.x), hi16(vc.x), lo16(vc.y), hi16(vc.y)};
    float kv[9];
#pragma unroll
    for (int e = 0; e < 9; ++e) kv[e] = (kk[e] * c3 + kk[e + 1] * c2 + kk[e + 2] * c1 + kk[e + 3] * c0) * vv[e];
    st128(xw - 1024 * r, (u4){pk(kv[8], kv[7]), pk(kv[6], kv[5]), pk(kv[4], kv[3]), pk(kv[2], kv[1])});
    st128(xw - 1024 * r + A_XB, (u4){pk(kv[7], kv[6]), pk(kv[5], kv[4]), pk(kv[3], kv[2]), pk(kv[1], kv[0])});
  };
  auto fin = [&](int r) {
    u2 ya = ldtr(yr), yb = ldtr(yr + 4 * A_YS);
    u4 o = {pk(lo16(qv.x) * lo16(ya.x), hi16(qv.x) * hi16(ya.x)), pk(lo16(qv.y) * lo16(ya.y), hi16(qv.y) * hi16(ya.y)),
            pk(lo16(qv.z) * lo16(yb.x), hi16(qv.z) * hi16(yb.x)), pk(lo16(qv.w) * lo16(yb.y), hi16(qv.w) * hi16(yb.y))};
    __builtin_amdgcn_raw_buffer_store_b128(o, bo, (prow + r * 512) * (D_ * 4) + oct * 16, 0, 16);
  };
#define A_ROUND(R)                                                                                         \
  WAITV(0);                                                                                                \
  bar();                                                                                                   \
  ST(1 + 3 * R);                                                                                           \
  if (R == 0) asm volatile("" ::"v"(fd));                                                                  \
  if (R > 0) fin(R - 1);                                                                                   \
  prep(R);                                                                                                 \
  bar();                                                                                                   \
  ST(2 + 3 * R);                                                                                           \
  if (R < 3) issue_raw(R + 1);                                                                             \
  qv = __builtin_amdgcn_raw_buffer_load_b128(bq, prow * (D_ * 2) + oct * 16, R * 512 * (D_ * 2), AUX_A);   \
  conv_phase<R>(acc, xa, xcap, ha, fd);                                                                    \
  st64(yw, (u2){pk(acc[2 * R][0], acc[2 * R][1]), pk(acc[2 * R][2], acc[2 * R][3])});                      \
  st64(yw + 512, (u2){pk(acc[2 * R + 1][0], acc[2 * R + 1][1]), pk(acc[2 * R + 1][2], acc[2 * R + 1][3])}); \
  ST(3 + 3 * R);
  A_ROUND(0) A_ROUND(1) A_ROUND(2) A_ROUND(3)
  bar();
  fin(3);
  ST(13);
}

constexpr u32 B_XB = 14 * 1024, B_SLOT = 41 * 1024;
constexpr u32 B_IMG = LDS_BYTES - 112 * 432;
constexpr u32 B_WO = 2 * B_SLOT - 72 * 1024;
constexpr u32 B_OT = 2 * B_SLOT;
static_assert(B_OT + 8 * 2304 <= B_IMG, "lds");

struct BDma {
  u4 rx, rw, r1;
  u32 v[6], m1, sm;
  int w;
  DEV void issue(int stage, u32 slot) const {
    const u32 d = sm + slot + w * 1024, sw = stage * (64 * 3456);
    dma16<AUX_B>(rx, v[0], stage * 128, d);
    dma16<AUX_B>(r1, v[1], stage * m1, d + 8192);
    dma16<AUX_B>(rw, v[2], sw, d + 2 * 8192);
    dma16<AUX_B>(rw, v[3], sw, d + 3 * 8192);
    dma16<AUX_B>(rw, v[4], sw, d + 4 * 8192);
    if (w == 0) dma16<AUX_B>(rw, v[5], sw, d + 5 * 8192);
  }
};

template <int NR, int NC, class F>
DEV void qkv_gemm(const BDma& dm, int st0, int rb0, int ct0, F&& late) {
  const int l = threadIdx.x & 63, r = l & 15, g = l >> 4;
  const u32 sm = dm.sm;
  const u32 xa = sm + (rb0 * 16 + r) * 128 + ((g ^ (r >> 1)) * 16);
  const u32 wa = sm + B_XB + (16 * (g >> 1) + 2 * ((r >> 2) + 4 * (g & 1))) * 432 + ct0 * 32 + (l & 3) * 8;
  f4 acc[NR][NC];
#pragma unroll
  for (int a = 0; a < NR; ++a)
#pragma unroll
    for (int c = 0; c < NC; ++c) acc[a][c] = (f4){0.f, 0.f, 0.f, 0.f};
  constexpr int NM = NR * NC, NL = 2 * NC + NR;
  u4 xf[2][NR];
  u2 wf[2][NC][2];
  u32 xs, ws;
  auto rd = [&](int s, int i) {
    if (i == 2) xf[s][0] = ld128(xs ^ (s * 64));
    else if (i <= 2 * NC) {
      const int q = i < 2 ? i : i - 1;
      wf[s][q >> 1][q & 1] = ldtr(ws + s * 13824 + (q >> 1) * 32 + (q & 1) * 432);
    } else xf[s][i - 2 * NC] = ld128((xs ^ (s * 64)) + (i - 2 * NC) * 2048);
  };
  auto step = [&](int s, bool more, auto&& mid) {
#pragma unroll
    for (int m = 0; m < NM; ++m) {
      const int a = m / NC, c = m % NC;
      acc[a][c] = mma(cat(wf[s][c][0], wf[s][c][1]), xf[s][a], acc[a][c]);
      if (more) {
#pragma unroll
        for (int i = 2 * m; i < 2 * m + 2 && i < NL; ++i) rd(s ^ 1, i);
      }
      if (m == (NL + 1) / 2) mid();
      SB();
    }
  };
  u32 slot = B_SLOT;
  int st = st0 + 3;
  xs = xa + slot;
  ws = wa + slot;
#pragma unroll
  for (int i = 0; i < NL; ++i) rd(0, i);
  SB();
#pragma unroll 1
  for (int jj = 0; jj < 8; ++jj) {
    step(0, true, [] {});
    if (jj < 7) WAITV(5); else WAITV(0);
    bar();
    const u32 freed = slot;
    slot = slot == 2 * B_SLOT ? 0 : slot + B_SLOT;
    xs = xa + slot;
    ws = wa + slot;
    SB();
    step(1, true, [&] {
      if (jj < 6) {
        if (st >= 9) st -= 9;
        dm.issue(st, freed);
        ++st;
      } else if (jj == 7) {
        late();
      }
    });
  }
  step(0, true, [] {});
  step(1, false, [] {});
  const u32 ia = sm + B_IMG + (rb0 * 16 + r) * 432 + ct0 * 32 + g * 8;
#pragma unroll
  for (int a = 0; a < NR; ++a)
#pragma unroll
    for (int c = 0; c < NC; ++c) {
      u2 o = {pk(acc[a][c][0], acc[a][c][1]), pk(acc[a][c][2], acc[a][c][3])};
      if (c < NC - 1 || ct0 + c < 13 || g < 2) st64(ia + a * (16 * 432) + c * 32, o);
    }
}

DEV void role_b(u64& probe_t, const Params& p, u32 sm, int xcd, int i) {
  const int t = threadIdx.x, l = t & 63, w = uni(t >> 6);
  const int rt = 5 * (xcd >> 1) + i % 5, grp = 4 * (xcd & 1) + i / 5;
  const int par = rt / 10, sg0 = 104 * (rt % 10);
  const auto bw = mk_br(p.ws, WS_USED);
  BDma dm;
  dm.rx = mk_rs(p.x, TBYTES);
  dm.rw = mk_rs(p.w, 3 * D_ * D_ * 2);
  dm.sm = sm;
  dm.w = w;
  dm.r1 = w < 6 ? dm.rx : dm.rw;
  dm.m1 = w < 6 ? 128 : 64 * 3456;
#pragma unroll
  for (int u = 0; u < 6; ++u) {
    const int pc = w + 8 * u;
    const int row = 8 * pc + (l >> 3), sg = sg0 - 8 + row;
    const int s = sg < 0 ? 0 : 2 * min(sg, 1023) + par;
    const u32 vx = s * (D_ * 2) + (((l & 7) ^ ((row >> 1) & 7)) * 16);
    const int n = 64 * (pc - 14) + l, ro = n / 27, c = n % 27;
    const int k = 16 * (ro >> 4) + 8 * ((ro >> 3) & 1) + 4 * (ro & 1) + ((ro >> 1) & 3);
    const u32 vw = k * 3456 + (c / 9) * 1152 + grp * 144 + (c % 9) * 16;
    dm.v[u] = u == 0 ? vx : (u == 1 ? (w < 6 ? vx : vw) : vw);
  }
  int st0 = (2 * (i % 5) + 2 * (i / 5)) % 9;
  dm.issue(st0, B_SLOT);
  dm.issue(st0 == 8 ? 0 : st0 + 1, 2 * B_SLOT);
  dm.issue(st0 >= 7 ? st0 - 7 : st0 + 2, 0);
  const bool has2 = i < 18;
  const int rt2 = 2 * xcd + i / 9, cg = i % 9;
  const u4 rwo = mk_rs(p.wo, D_ * D_ * 2);
  const u32 wsw = cg * 128 + (((l & 7) ^ (((l >> 4) & 3) << 1)) * 16);
  auto wo_piece = [&](int u) {
    const int P = w + 8 * u, e = P >> 2, tt = P & 1, gh = (P >> 1) & 1;
    if (u < 8) dma16<AUX_B>(rwo, (l >> 3) * 1152 + wsw, (72 * (e >> 1) + 32 * (e & 1) + 16 * tt + 8 * gh) * 1152, sm + B_WO + P * 1024);
    else dma16<AUX_B>(rwo, (72 * (l >> 5) + ((l >> 3) & 3)) * 1152 + wsw, (72 * (4 * (e - 16) + 2 * gh) + 64 + 4 * tt) * 1152, sm + B_WO + P * 1024);
  };
  auto late = [&]() {
    if (has2) {
#pragma unroll
      for (int u = 4; u < 9; ++u) wo_piece(u);
    }
  };
  ST(1);
  WAITV(10);
  bar();
  ST(2);
  {
    const int rb0 = w < 4 ? 0 : 4, cq = (w + (w >> 2) * 2) & 3;
    const int ct0 = cq < 2 ? 4 * cq : 3 * cq + 2;
    if (w < 2) qkv_gemm<4, 4>(dm, st0, rb0, ct0, late);
    else if (w < 4) qkv_gemm<4, 3>(dm, st0, rb0, ct0, late);
    else if (w < 6) qkv_gemm<3, 3>(dm, st0, rb0, ct0, late);
    else qkv_gemm<3, 4>(dm, st0, rb0, ct0, late);
  }
  ST(3);
  if (w == 7) {
    const u2 e2 = __builtin_amdgcn_raw_buffer_load_b64(bw, WS_EPOCH, 0, 16);
    const u64 ep = (((u64)e2.y << 32) | e2.x) + 1;
    if (l == 0) __builtin_amdgcn_raw_buffer_store_b64((u2){(u32)ep, (u32)(ep >> 32)}, bw, WS_FLAG + 256 + (xcd * 20 + i) * 8, 0, 16);
    for (int it = 0; it < SPIN_CAP; ++it) {
      asm volatile("" ::: "memory");
      const u2 f = __builtin_amdgcn_raw_buffer_load_b64(bw, WS_FLAG + min(l, 23) * 8, 0, 16);
      if (__all(f.x == (u32)ep && f.y == (u32)(ep >> 32))) break;
      __builtin_amdgcn_s_sleep(8);
    }
  }
  bar();
  ST(4);
  if (has2) {
#pragma unroll
    for (int u = 0; u < 4; ++u) wo_piece(u);
  }
  const int c = l & 15, g = l >> 4;
  if (w < 7) {
    const u32 im = sm + B_IMG;
    const u32 qa = im + min(8 + 16 * w + c, 111) * 432 + g * 16;
    const u32 ka0 = im + (16 * w + c) * 432 + 144 + g * 16;
    const u32 ka1 = im + min(16 * w + 16 + c, 111) * 432 + 144 + g * 16;
    const u32 va0 = im + (16 * w + 4 * g + (c >> 2)) * 432 + 288 + (l & 3) * 8;
    const u32 va1 = im + min(16 * w + 16 + 4 * g + (c >> 2), 111) * 432 + 288 + (l & 3) * 8;
    const int kfirst = 8 - sg0 - 16 * w;
    const float cs = 0.23570226039551584f * 1.4426950408889634f, neg = -1e9f * 1.4426950408889634f;
    u4 vf[5];
#pragma unroll
    for (int ct = 0; ct < 5; ++ct) vf[ct] = cat(ldtr(va0 + ct * 32), ldtr(va1 + ct * 32));
    u2 ot[5];
#pragma unroll
    for (int hh = 0; hh < 4; ++hh) {
      u4 qf = ld128(qa + hh * 32), k0 = ld128(ka0 + hh * 32), k1 = ld128(ka1 + hh * 32);
#pragma unroll
      for (int d = 0; d < 4; ++d) {
        const bool in = (u32)(4 * g + d - hh) < 9u;
        qf[d] = in ? qf[d] : 0u;
        k0[d] = in ? k0[d] : 0u;
        k1[d] = in ? k1[d] : 0u;
      }
      const f4 z = {0.f, 0.f, 0.f, 0.f};
      const f4 s0 = mma(k0, qf, z), s1 = mma(k1, qf, z);
      float sv[8], m = -INFINITY;
#pragma unroll
      for (int e = 0; e < 8; ++e) {
        const int kap = 16 * (e >> 2) + 4 * g + (e & 3);
        float x = (e < 4 ? s0[e & 3] : s1[e & 3]) * cs;
        if (e < 4) x = kap < kfirst ? neg : x;
        sv[e] = (u32)(kap - c - 1) < 8u ? x : -INFINITY;
        m = fmaxf(m, sv[e]);
      }
      m = rows_max(m);
      float sum = 0.f;
#pragma unroll
      for (int e = 0; e < 8; ++e) {
        sv[e] = __builtin_amdgcn_exp2f(sv[e] - m);
        sum += sv[e];
      }
      const float inv = __builtin_amdgcn_rcpf(rows_sum(sum));
      const u4 pf = {pk(sv[0] * inv, sv[1] * inv), pk(sv[2] * inv, sv[3] * inv), pk(sv[4] * inv, sv[5] * inv), pk(sv[6] * inv, sv[7] * inv)};
      const f4 oa = mma(vf[hh], pf, z), ob = mma(vf[hh + 1], pf, z);
      const u2 a2 = {pk(oa[0], oa[1]), pk(oa[2], oa[3])};
      if (hh == 0) ot[0] = a2;
      else {
        ot[hh].x = 2 * g < hh ? ot[hh].x : a2.x;
        ot[hh].y = 2 * g + 1 < hh ? ot[hh].y : a2.y;
      }
      ot[hh + 1] = (u2){pk(ob[0], ob[1]), pk(ob[2], ob[3])};
    }
#pragma unroll
    for (int ct = 0; ct < 5; ++ct) {
      ot[ct].x = min(ot[ct].x, 0xfffffffeu);
      ot[ct].y = min(ot[ct].y, 0xfffffffeu);
    }
    const int qi = 16 * w + c, rho = par * 1024 + sg0 + qi;
    if (qi < 104 && sg0 + qi < 1024) {
      const u32 ob = WS_OF + (rho >> 4) * (18 * 1024) + (rho & 15) * 16;
      __builtin_amdgcn_raw_buffer_store_b128(cat(ot[0], ot[1]), bw, ob + g * 256 + 2 * grp * 1024, 0, 16);
      __builtin_amdgcn_raw_buffer_store_b128(cat(ot[2], ot[3]), bw, ob + g * 256 + (2 * grp + 1) * 1024, 0, 16);
      if (g < 2) __builtin_amdgcn_raw_buffer_store_b64(ot[4], bw, ob + g * 8 + (16 + (grp >> 2)) * 1024 + (grp & 3) * 256, 0, 16);
    }
  }
  ST(5);
  if (!has2) return;
  WAITV(0);
  bar();
  ST(6);
  {
    const u32 ob = WS_OF + (8 * rt2 + w) * (18 * 1024) + l * 16;
    constexpr int NPRE = 6;
    const int f = (2 * (g & 1) + (c >> 3)) & 3;
    u32 wb[4];
    u2 wv[NPRE][4][2];
#pragma unroll
    for (int ct = 0; ct < 4; ++ct) {
      wb[ct] = sm + B_WO + 16384 + (16 * (g >> 1) + 4 * (g & 1) + (c >> 2)) * 128 + (l & 3) * 8 + ((ct ^ f) * 32);
      asm volatile("" : "+v"(wb[ct]));
      __builtin_assume(wb[ct] < 0x10000u);
    }
    auto pre = [&]() {
#pragma unroll
      for (int ct = 0; ct < 4; ++ct) {
        u32 lo = wb[ct] - 16384;
        asm volatile("" : "+v"(lo));
        __builtin_assume(lo < 0x10000u);
#pragma unroll
        for (int e = 0; e < NPRE; ++e) {
          wv[e][ct][0] = ldtr(lo + e * 4096);
          wv[e][ct][1] = ldtr(lo + e * 4096 + 1024);
        }
      }
    };
    pre();
    SB();
    for (int it = 0; it < SPIN_CAP; ++it) {
      asm volatile("" ::: "memory");
      const u4 a = __builtin_amdgcn_raw_buffer_load_b128(bw, ob, 16 * 1024, 16);
      const u4 b = __builtin_amdgcn_raw_buffer_load_b128(bw, ob, 17 * 1024, 16);
      const u32 mx = max(max(max(a.x, a.y), max(a.z, a.w)), max(max(b.x, b.y), max(b.z, b.w)));
      if (__all(mx != 0xffffffffu)) break;
      __builtin_amdgcn_s_sleep(8);
    }
    ST(7);
    f4 acc[4];
    for (int it = 0; it < SPIN_CAP; ++it) {
      asm volatile("" ::: "memory");
      u4 of[18];
#pragma unroll
      for (int e = 0; e < 18; ++e) of[e] = __builtin_amdgcn_raw_buffer_load_b128(bw, ob, e * 1024, 16);
#pragma unroll
      for (int ct = 0; ct < 4; ++ct) acc[ct] = (f4){0.f, 0.f, 0.f, 0.f};
      u32 mx = 0;
      SB();
#pragma unroll
      for (int e = 0; e < 18; ++e) {
#pragma unroll
        for (int ct = 0; ct < 4; ++ct) {
          acc[ct] = mma(cat(wv[e % NPRE][ct][0], wv[e % NPRE][ct][1]), of[e], acc[ct]);
          if (e + NPRE < 18) {
            wv[e % NPRE][ct][0] = ldtr(wb[ct] + (e + NPRE) * 4096 - 16384);
            wv[e % NPRE][ct][1] = ldtr(wb[ct] + (e + NPRE) * 4096 - 16384 + 1024);
          }
          if (ct & 1) mx = max(mx, max(of[e][ct], of[e][ct - 1]));
          SB();
        }
      }
      if (__all(mx != 0xffffffffu)) break;
      pre();
    }
    ST(8);
    const u32 tb = sm + B_OT + w * 2304;
#pragma unroll
    for (int ct = 0; ct < 4; ++ct) st64(tb + c * 144 + ct * 32 + g * 8, (u2){pk(acc[ct][0], acc[ct][1]), pk(acc[ct][2], acc[ct][3])});
    const auto bo = mk_br(p.out, 2 * TBYTES);
#pragma unroll
    for (int hf = 0; hf < 2; ++hf) {
      const int rr = 8 * hf + (l >> 3), rho = (8 * rt2 + w) * 16 + rr;
      const u4 o = ld128(tb + rr * 144 + (l & 7) * 16);
      __builtin_amdgcn_raw_buffer_store_b128(o, bo, (2 * (rho & 1023) + (rho >> 10)) * (D_ * 4) + D_ * 2 + cg * 128 + (l & 7) * 16, 0, 16);
    }
    ST(9);
  }
}

DEV void role_s(u64& probe_t, const Params& p, int si) {
  const int t = threadIdx.x;
  const auto bw = mk_br(p.ws, WS_USED);
  const u2 e2 = __builtin_amdgcn_raw_buffer_load_b64(bw, WS_EPOCH, 0, 16);
#pragma unroll
  for (int u = 0; u < 12; ++u)
    __builtin_amdgcn_raw_buffer_store_b128((u4){~0u, ~0u, ~0u, ~0u}, bw, WS_OF + si * 98304 + t * 16 + u * 8192, 0, 16);
  WAITV(0);
  bar();
  const u64 ep = (((u64)e2.y << 32) | e2.x) + 1;
  const u2 ev = {(u32)ep, (u32)(ep >> 32)};
  if (t == 0) __builtin_amdgcn_raw_buffer_store_b64(ev, bw, WS_FLAG + si * 8, 0, 16);
  ST(1);
  if (si == 0 && t < 64) {
    for (int it = 0; it < SPIN_CAP; ++it) {
      __builtin_amdgcn_s_sleep(64);
      asm volatile("" ::: "memory");
      const u2 a = __builtin_amdgcn_raw_buffer_load_b64(bw, WS_FLAG + min(t, 23) * 8, 0, 16);
      const u4 b = __builtin_amdgcn_raw_buffer_load_b128(bw, WS_FLAG + 256 + t * 16, 0, 16);
      const u4 d = __builtin_amdgcn_raw_buffer_load_b128(bw, WS_FLAG + 256 + 1024 + min(t, 15) * 16, 0, 16);
      const bool ok = a.x == ev.x && b.x == ev.x && b.z == ev.x && d.x == ev.x && d.z == ev.x &&
                      a.y == ev.y && b.y == ev.y && b.w == ev.y && d.y == ev.y && d.w == ev.y;
      if (__all(ok)) break;
    }
    if (t == 0) __builtin_amdgcn_raw_buffer_store_b64(ev, bw, WS_EPOCH, 0, 16);
  }
}

__global__ __launch_bounds__(512, 2) void fused_kernel(Params p) {
  __shared__ __attribute__((aligned(1024))) char smem[LDS_BYTES];
  const u32 sm = (u32)(size_t)(LAS char*)smem;
  const int b = blockIdx.x, xcd = b & 7, i = b >> 3;
  const u64 probe_t0 = __builtin_amdgcn_s_memrealtime(); u64 probe_t = probe_t0;
  if (i < 20) role_b(probe_t, p, sm, xcd, i);
  else if (i < 28 || xcd < 2) role_a(probe_t, p, sm, i < 28 ? 8 * xcd + i - 20 : 64 + 4 * xcd + i - 28);
  else role_s(probe_t, p, (xcd - 2) * 4 + i - 28);
  if (blockIdx.x == 40 && threadIdx.x < 64) { const u64 lim = 4000 + 8 * (probe_t - probe_t0); while (__builtin_amdgcn_s_memrealtime() - probe_t0 < lim) __builtin_amdgcn_s_sleep(1); }
}

extern "C" void kernel_launch(void* const* d_in, const int* in_sizes, int n_in, void* d_out, int out_size,
                              void* d_ws, size_t ws_size, hipStream_t stream) {
  Params p;
  p.q = (const u16*)d_in[0];
  p.k = (const u16*)d_in[1];
  p.v = (const u16*)d_in[2];
  p.sk = (const u16*)d_in[3];
  p.h = (const u16*)d_in[4];
  p.dd = (const u16*)d_in[5];
  p.x = (const u16*)d_in[6];
  p.w = (const u16*)d_in[7];
  p.wo = (const u16*)d_in[8];
  p.out = (u16*)d_out;
  p.ws = (char*)d_ws;
  fused_kernel<<<256, 512, 0, stream>>>(p);
}
```

```cpp
#include <hip/hip_runtime.h>
#include <hip/hip_bf16.h>

typedef unsigned short u16;
typedef unsigned int u32;
typedef unsigned long long u64;
typedef __bf16 bf8 __attribute__((ext_vector_type(8)));
typedef __bf16 bf2 __attribute__((ext_vector_type(2)));
typedef short s4 __attribute__((ext_vector_type(4)));
typedef float f2 __attribute__((ext_vector_type(2)));
typedef float f4 __attribute__((ext_vector_type(4)));
typedef u32 u2 __attribute__((ext_vector_type(2)));
typedef u32 u4 __attribute__((ext_vector_type(4)));
#define LAS __attribute__((address_space(3)))
#define DEV __device__ __forceinline__

constexpr int S_ = 2048, D_ = 576;
constexpr u32 TBYTES = S_ * D_ * 2;
constexpr u32 WS_FLAG = 0, WS_EPOCH = 8192, WS_OF = 65536, WS_USED = WS_OF + TBYTES;
constexpr int SPIN_CAP = 1 << 16;
constexpr int LDS_BYTES = 163840;
constexpr u32 LDS_DUMMY = LDS_BYTES - 256;
#define CODE_SZ 31208
constexpr u32 CODE_BYTES = CODE_SZ;
constexpr int G1 = 3;
constexpr u32 OOB = 0x80000000u;

#define ST(n)
#define NRETRY
#define ST_BEGIN
#define ST_END

struct Params {
  const u16 *q, *k, *v, *sk, *h, *dd, *x, *w, *wo;
  u16* out;
  char* ws;
};

enum { KQ, KK, KV, KSK, KH, KDD, KX, KW, KWO, KOUT, KWS };
template <int N>
DEV char* karg() {
  u64 r;
  asm volatile("s_mov_b64 %0, s[%c1:%c2]" : "=s"(r) : "n"(76 + 2 * N), "n"(77 + 2 * N));
  return (char*)r;
}
DEV u4 mk_rs(const void* p, u32 bytes) {
  u64 a = (u64)p;
  return (u4){(u32)a, (u32)(a >> 32) & 0xffffu, bytes, 0x00020000u};
}
DEV __amdgpu_buffer_rsrc_t mk_br(const void* p, u32 bytes) {
  return __builtin_amdgcn_make_buffer_rsrc((void*)p, 0, bytes, 0x00020000);
}
template <int AUX>
DEV void dma16(u4 rs, u32 voff, u32 soff, u32 lds) {
  if constexpr (AUX == 2)
    asm volatile("s_mov_b32 m0, %3\n\ts_nop 0\n\tbuffer_load_dwordx4 %0, %1, %2 offen nt lds" ::"v"(voff), "s"(rs), "s"(soff), "s"(lds) : "memory");
  else
    asm volatile("s_mov_b32 m0, %3\n\ts_nop 0\n\tbuffer_load_dwordx4 %0, %1, %2 offen lds" ::"v"(voff), "s"(rs), "s"(soff), "s"(lds) : "memory");
}
#define WAITV(n) asm volatile("s_waitcnt vmcnt(" #n ")" ::: "memory")
#define SB() __builtin_amdgcn_sched_barrier(0)
#define WAITL() asm volatile("s_waitcnt lgkmcnt(0)" ::: "memory")
DEV void bar() { asm volatile("s_waitcnt lgkmcnt(0)\n\ts_barrier" ::: "memory"); }
DEV u4 ld128(u32 a) { return *(const LAS u4*)(size_t)a; }
DEV u2 ld64(u32 a) { return *(const volatile LAS u2*)(size_t)a; }
DEV void st128(u32 a, u4 v) { *(LAS u4*)(size_t)a = v; }
DEV void st64(u32 a, u2 v) { *(LAS u2*)(size_t)a = v; }
DEV void st32(u32 a, u32 v) { *(LAS u32*)(size_t)a = v; }
DEV u2 ldtr(u32 a) { return __builtin_bit_cast(u2, __builtin_amdgcn_ds_read_tr16_b64_v4i16((LAS s4*)(size_t)a)); }
DEV u4 cat(u2 a, u2 b) { return (u4){a.x, a.y, b.x, b.y}; }
DEV f4 mma(u4 a, u4 b, f4 c) {
  return __builtin_amdgcn_mfma_f32_16x16x32_bf16(__builtin_bit_cast(bf8, a), __builtin_bit_cast(bf8, b), c, 0, 0, 0);
}
DEV void mmx(f4& c, u4 a, u4 b) { asm volatile("v_mfma_f32_16x16x32_bf16 %0, %1, %2, %0" : "+v"(c) : "v"(a), "v"(b)); }
DEV void mm0(f4& c, u4 a, u4 b) { asm volatile("v_mfma_f32_16x16x32_bf16 %0, %1, %2, 0" : "=&v"(c) : "v"(a), "v"(b)); }
#define OPQ(x) asm volatile("" : "+v"(x))
#define NOPS(n) asm volatile("s_nop " #n)
DEV u32 pk(float lo, float hi) { return __builtin_bit_cast(u32, __builtin_convertvector((f2){lo, hi}, bf2)); }
DEV u32 pm(float a, float b) { return min(pk(a, b), 0xfffffffeu); }
DEV float lo16(u32 x) { return __uint_as_float(x << 16); }
DEV float hi16(u32 x) { return __uint_as_float(x & 0xffff0000u); }
DEV u32 uni(u32 x) { return __builtin_amdgcn_readfirstlane(x); }
DEV u2 tag_next(u2 e) {
  const u64 t = (((u64)e.y << 32) | e.x) + 1;
  return (u2){(u32)t, (u32)(t >> 32)};
}
DEV bool tag_all(u4 f, u2 e) { return __all(((f.x ^ e.x) | (f.y ^ e.y) | (f.z ^ e.x) | (f.w ^ e.y)) == 0); }
DEV float fmx(float a, float b) { return __builtin_elementwise_maximum(a, b); }
DEV void swap16(float& a, float& b) {
  auto r = __builtin_amdgcn_permlane16_swap(__float_as_uint(a), __float_as_uint(b), false, false);
  a = __uint_as_float(r[0]);
  b = __uint_as_float(r[1]);
}
DEV void swap32(float& a, float& b) {
  auto r = __builtin_amdgcn_permlane32_swap(__float_as_uint(a), __float_as_uint(b), false, false);
  a = __uint_as_float(r[0]);
  b = __uint_as_float(r[1]);
}
DEV float rows_max(float (&v)[4]) {
  swap16(v[0], v[1]);
  swap16(v[2], v[3]);
  float a = fmx(v[0], v[1]), b = fmx(v[2], v[3]);
  swap32(a, b);
  return fmx(a, b);
}
DEV void rows_spread(float t, float (&v)[4]) {
  v[0] = v[2] = t;
  swap32(v[0], v[2]);
  v[1] = v[0];
  v[3] = v[2];
  swap16(v[0], v[1]);
  swap16(v[2], v[3]);
}

DEV u32 kstage(u32 xcd, u32 st) {
  return (u32)((((xcd ^ (xcd >> 1)) & 1) ? 0x432108765ull : 0x876543210ull) >> (4 * st)) & 15;
}

DEV void touch_lines(u4 rs, u32 v, u32 so, u32 lds) {
  asm volatile("s_mov_b32 m0, %3\n\ts_nop 0\n\tbuffer_load_dword %0, %1, %2 offen lds" ::"v"(v), "s"(rs), "s"(so), "s"(lds) : "memory");
}

constexpr u32 A_R = 0, A_RV = 68 * 16, A_RS = A_RV + 1024;
constexpr u32 A_H = 12 * A_RS, A_HS = 8256, A_HB = 4128;
constexpr u32 A_X = A_H + 8 * A_HS, A_XS = 8400, A_XB = 4224;
static_assert(A_X + 8 * A_XS <= LDS_DUMMY, "lds");

struct ConvRegs {
  u2 g[2][2][2][2];
  u4 f[2][8];
};
template <int R>
DEV void conv_round(f4 (&acc)[2][8], ConvRegs& c, u32 xa, const u32 ha0, u32 hmin) {
  constexpr int NA = 8 - 2 * R, NM = 2 * NA - 1;
  auto rdg = [&](int ch, int s, int i, u32 x) { c.g[ch][s][i >> 1][i & 1] = ld64(x + ch * A_XS + (i < 2 ? 512 : 0) + 8 * (i & 1)); };
  OPQ(xa);
#pragma unroll
  for (int i = 0; i < 8; ++i) rdg(i >> 2, 0, i & 3, xa);
  xa -= 128;
  u32 ha = ha0 - 128, hm64 = hmin - 64, h0 = max(ha, hm64);
  OPQ(xa); OPQ(ha); OPQ(h0); OPQ(hm64); OPQ(hmin);
  __builtin_amdgcn_s_waitcnt(0xC47F);
  SB();
  auto step = [&](int ch, int s, bool last, u32 x, u32 h, u32 hz) {
#pragma unroll
    for (int m = 0; m < NM; ++m) {
      const int a = m >> 1, o = m & 1;
      mmx(acc[ch][2 * R + a + o], c.f[ch][a], cat(c.g[ch][s][o][0], c.g[ch][s][o][1]));
      SB();
      if ((o || m == NM - 1) && (!last || a < NA - 2)) c.f[ch][a] = ld128((a ? h + 512 * a : hz) + ch * A_HS);
      if (!last) {
        if (R < 2) { if (!o && a < 4) rdg(ch, s ^ 1, a, x); }
        else if (R == 2) { if (!o && a < 3) rdg(ch, s ^ 1, a, x); if (m == 0) rdg(ch, s ^ 1, 3, x); }
        else { rdg(ch, s ^ 1, m, x); if (m == 0) rdg(ch, s ^ 1, 3, x); }
      }
      SB();
    }
  };
#pragma unroll 1
  for (int b = 0; b < 3; ++b) {
    step(0, 0, false, xa + 64, ha + 64, h0 + 64);
    step(1, 0, false, xa + 64, ha + 64, h0 + 64);
    h0 = max(ha, hmin);
    OPQ(h0);
    step(0, 1, false, xa, ha, h0);
    step(1, 1, false, xa, ha, h0);
    xa -= 128;
    ha -= 128;
    h0 = max(ha, hm64);
    OPQ(xa); OPQ(ha); OPQ(h0);
  }
  step(0, 0, false, xa + 64, ha + 64, h0 + 64);
  step(1, 0, false, xa + 64, ha + 64, h0 + 64);
  step(0, 1, true, 0, ha0, ha0);
  step(1, 1, true, 0, ha0, ha0);
  if (R == 2) NOPS(3);
  if (R == 3) NOPS(7);
#pragma unroll
  for (int i = 0; i < 4; ++i) OPQ(acc[i >> 1][2 * R + (i & 1)]);
}

DEV void role_a(u32 sm, int oct) {
  const int ra = oct < 64 ? oct & 7 : 2 * (oct & 3);
  const int t = threadIdx.x, l = t & 63, w = uni(t >> 6), hw = w - 4;
  const int j = l & 15, g = l >> 4;
  const u32 vrow = l * (D_ * 2) + oct * 16;
  const int ch = oct * 8 + w, un0 = (w + ra) & 7;
  const u32 hb = sm + A_H + w * A_HS, rb0 = sm + A_R + (3 * (w & 3) + 1 - (w >> 2)) * A_RS;
  WAITL();
  const u4 rk = mk_rs(karg<KK>(), TBYTES), rv = mk_rs(karg<KV>(), TBYTES), rh = mk_rs(karg<KH>(), TBYTES);
  u2 skr;
  u32 dv;
  asm volatile("buffer_load_dwordx2 %0, %2, %3, 0 offen\n\tbuffer_load_ushort %1, %4, %5, 0 offen"
               : "=&v"(skr), "=&v"(dv)
               : "v"((oct * 8 + (l & 7)) * 8), "s"(mk_rs(karg<KSK>(), D_ * 8)), "v"(ch * 2), "s"(mk_rs(karg<KDD>(), D_ * 2)) : "memory");
  const u4 r1 = (oct & 1) ? rv : rk, r2 = (oct & 1) ? rk : rv;
  const u32 o1 = (oct & 1) ? A_RV : 64, o2 = A_RV + 64 - o1;
  auto issue_unit = [&](int un, u32 buf) {
    dma16<0>(r1, vrow, un * 64 * (D_ * 2), buf + o1);
    dma16<0>(r2, vrow, un * 64 * (D_ * 2), buf + o2);
    const u32 vh = un ? vrow + (un * 64 - 4) * (D_ * 2) : OOB;
    if (l < 4) dma16<0>(rk, vh, 0, buf);
  };
  auto seq = [&](int n) {
    constexpr u32 t8[6] = {0x0a091008u, 0x0e0d0c0bu, 0x1812110fu, 0x16151413u, 0x1b1a1917u, 0x1f1e1d1cu};
    constexpr u32 t4[6] = {0x11100908u, 0x0d0c0b0au, 0x19180f0eu, 0x15141312u, 0x1b1a1716u, 0x1f1e1d1cu};
    const u32 c = (oct < 64 ? t8[n] : t4[n]) >> (8 * hw);
    return (int)((c & 24) | ((c + ra) & 7));
  };
  issue_unit(un0, rb0);
  asm volatile("s_barrier" ::: "memory");
#pragma unroll
  for (int i = 0; i < 4; ++i) dma16<2>(rh, l * 16, ch * 4096 + i * 1024, hb + 32 + i * 1024);
  if (l < 16) {
    st32(hb + (l >> 3) * A_HB + (l & 7) * 4, 0);
    st32(sm + A_X + w * A_XS + 4096 + l * 4, 0);
    st32(sm + A_X + w * A_XS + A_XB + 4100 + l * 4, 0);
  }
  asm volatile("s_waitcnt vmcnt(4)" : "+v"(skr), "+v"(dv)::"memory");
  auto copy_h = [&]() {
    u4 d[4];
    u32 pv[4];
#pragma unroll
    for (int i = 0; i < 4; ++i) {
      d[i] = ld128(hb + 32 + i * 1024 + l * 16);
      pv[i] = *(const LAS u32*)(size_t)(hb + 28 + i * 1024 + l * 16);
    }
    if (l == 0) {
      d[0].x = pk(lo16(d[0].x) + lo16(dv), hi16(d[0].x));
      st32(hb + 32, d[0].x);
    }
#pragma unroll
    for (int i = 0; i < 4; ++i)
      st128(hb + A_HB + 32 + i * 1024 + l * 16,
            (u4){__builtin_amdgcn_alignbit(d[i].x, pv[i], 16), __builtin_amdgcn_alignbit(d[i].y, d[i].x, 16),
                 __builtin_amdgcn_alignbit(d[i].z, d[i].y, 16), __builtin_amdgcn_alignbit(d[i].w, d[i].z, 16)});
  };
  const u32 tra = (16 * g + 8 * ((l >> 1) & 1) + (j >> 2)) * 16 + 8 * (l & 1);
  const u32 xw = sm + A_X + (l & 7) * A_XS + 2 * (2040 - 8 * (l >> 3));
  const float c0 = lo16(skr.x), c1 = hi16(skr.x), c2 = lo16(skr.y), c3 = hi16(skr.y);
  auto prep = [&](int un, u32 buf) {
    const u32 a = buf + tra;
    const u2 ka = ldtr(a), kb = ldtr(a + 64), kc = ldtr(a + 128), vb = ldtr(a + A_RV), vc = ldtr(a + A_RV + 64);
    const float kk[11] = {hi16(ka.x), lo16(ka.y), hi16(ka.y), lo16(kb.x), hi16(kb.x), lo16(kb.y),
                          hi16(kb.y), lo16(kc.x), hi16(kc.x), lo16(kc.y), hi16(kc.y)};
    const float vv[8] = {lo16(vb.x), hi16(vb.x), lo16(vb.y), hi16(vb.y), lo16(vc.x), hi16(vc.x), lo16(vc.y), hi16(vc.y)};
    float kv[8];
#pragma unroll
    for (int e = 0; e < 8; ++e) kv[e] = (kk[e] * c3 + kk[e + 1] * c2 + kk[e + 2] * c1 + kk[e + 3] * c0) * vv[e];
    const u4 d = {pk(kv[7], kv[6]), pk(kv[5], kv[4]), pk(kv[3], kv[2]), pk(kv[1], kv[0])};
    const u32 x = xw - 128 * un;
    st128(x, d);
    st32(x + A_XB + 4, d.x);
    st32(x + A_XB + 16, d.w);
    st64(x + A_XB + 8, (u2){d.y, d.z});
  };
  const u4 rq = mk_rs(karg<KQ>(), TBYTES);
  prep(un0, rb0);
  if (w >= 4) {
    issue_unit(seq(0), rb0 + 2 * A_RS);
    WAITV(3);
  } else {
    WAITV(0);
  }
  ST(1);
  copy_h();
  const auto bo = mk_br(karg<KOUT>(), 2 * TBYTES);
  const int prow = 2 * (l >> 2) + (l & 1) + 32 * ((l >> 1) & 1);
  const u32 yr = sm + A_H + 32 + 3072 + (j >> 2) * A_HS + 32 * g + 8 * (l & 3);
  const u32 vq = prow * (D_ * 2) + oct * 16;
  auto qload = [&](u4& d, int un) {
    asm volatile("buffer_load_dwordx4 %0, %1, %2, %3 offen" : "=&v"(d) : "v"(vq), "s"(rq), "s"(un * 64 * (D_ * 2)) : "memory");
  };
  const u32 vo = prow * (D_ * 4) + oct * 16;
  auto fin = [&](u4& qv, int un) {
    const u32 a = yr - 1024 * (un >> 3) + 128 * (un & 7);
    const u2 ya = ldtr(a), yb = ldtr(a + 4 * A_HS);
    qv = (u4){pk(lo16(qv.x) * lo16(ya.x), hi16(qv.x) * hi16(ya.x)), pk(lo16(qv.y) * lo16(ya.y), hi16(qv.y) * hi16(ya.y)),
              pk(lo16(qv.z) * lo16(yb.x), hi16(qv.z) * hi16(yb.x)), pk(lo16(qv.w) * lo16(yb.y), hi16(qv.w) * hi16(yb.y))};
    __builtin_amdgcn_raw_buffer_store_b128(qv, bo, vo + un * (64 * D_ * 4), 0, 0);
  };
  if (w < 4) {
    __builtin_amdgcn_s_setprio(1);
    const u32 hc = sm + A_H + 2 * w * A_HS + ((j & 1) ? 0 : A_HB);
    u32 ha = hc + 32 + 64 * (j >> 1) + 16 * g, hmin = hc + 16;
    u32 x3 = sm + A_X + 2 * w * A_XS + 4 * (1023 - j + 4 * g) + ((j & 1) ? 0 : A_XB + 4) - 512 - 3072;
    u32 y3 = sm + A_H + 32 + 2 * w * A_HS + l * 8;
    OPQ(ha); OPQ(x3); OPQ(y3);
    const u32 xa = x3 + 3072, yw = y3 + 3072;
    f4 acc[2][8];
#pragma unroll
    for (int n = 0; n < 16; ++n) {
      acc[n >> 3][n & 7] = (f4){0.f, 0.f, 0.f, 0.f};
      OPQ(acc[n >> 3][n & 7]);
    }
#define A_ROUND(R)                                                                                          \
  conv_round<R>(acc, cr, xa - 1024 * R, ha, hmin);                                                          \
  _Pragma("unroll") for (int i = 0; i < 4; ++i) {                                                           \
    const f4 y = acc[i >> 1][2 * R + (i & 1)];                                                              \
    st64(yw - 1024 * R + (i >> 1) * A_HS + (i & 1) * 512, (u2){pk(y[0], y[1]), pk(y[2], y[3])});            \
  }                                                                                                         \
  ST(3 + 2 * R);                                                                                            \
  bar();                                                                                                    \
  ST(4 + 2 * R);
    ConvRegs cr;
    bar();
    ST(2);
#pragma unroll
    for (int i = 0; i < 16; ++i) cr.f[i >> 3][i & 7] = ld128(ha + (i >> 3) * A_HS + (i & 7) * 512);
    A_ROUND(0) A_ROUND(1) A_ROUND(2) A_ROUND(3)
    u4 q0 = ld128(rb0 - A_RS + l * 16), q1 = ld128(rb0 - A_RS + 1024 + l * 16);
    fin(q0, 24 + w);
    fin(q1, 28 + w);
    ST(13);
  } else {
    u4 q[6];
    bar();
    ST(2);
    issue_unit(seq(1), rb0);
    issue_unit(seq(2), rb0 + A_RS);
    WAITV(6); prep(seq(0), rb0 + 2 * A_RS);
    touch_lines(rq, (hw < 2 || oct >= 64) ? vrow : OOB, (ra * 2 + hw) * 64 * (D_ * 2), sm + LDS_DUMMY);
    WAITV(4); prep(seq(1), rb0);
    WAITV(1); prep(seq(2), rb0 + A_RS);
    ST(3);
    bar();
    ST(4);
    issue_unit(seq(3), rb0 + 2 * A_RS);
    issue_unit(seq(4), rb0);
    WAITV(3); prep(seq(3), rb0 + 2 * A_RS);
    issue_unit(seq(5), rb0 + A_RS);
    qload(q[0], hw); qload(q[1], 4 + hw);
    WAITV(5); prep(seq(4), rb0);
    ST(5);
    bar();
    ST(6);
    qload(q[2], 8 + hw); qload(q[3], 12 + hw);
    WAITV(4); prep(seq(5), rb0 + A_RS);
    qload(q[4], 16 + hw); qload(q[5], 20 + hw);
    asm volatile("s_waitcnt vmcnt(4)" : "+v"(q[0]), "+v"(q[1])::"memory");
    fin(q[0], hw); fin(q[1], 4 + hw);
    ST(7);
    bar();
    ST(8);
    dma16<0>(rq, vq, (24 + hw) * 64 * (D_ * 2), rb0);
    dma16<0>(rq, vq, (28 + hw) * 64 * (D_ * 2), rb0 + 1024);
    asm volatile("s_waitcnt vmcnt(4)" : "+v"(q[2]), "+v"(q[3]), "+v"(q[4]), "+v"(q[5])::"memory");
    fin(q[2], 8 + hw); fin(q[3], 12 + hw);
    WAITV(2);
    ST(9);
    bar();
    ST(10);
    fin(q[4], 16 + hw); fin(q[5], 20 + hw);
    ST(13);
  }
}

constexpr u32 B_XB = 14 * 1024, B_SLOT = 41 * 1024;
constexpr u32 B_CS = 112 * 16, B_IMG = LDS_BYTES - 28 * B_CS;
constexpr u32 B_WO = B_SLOT - 36 * 1024;
constexpr u32 B_RED = B_WO + 72 * 1024;
constexpr u32 B_OT = B_IMG;
static_assert(B_RED + 32 * 1024 <= B_IMG, "lds");

template <int HI>
DEV void qkv_gemm(u32 sm, int cs) {
  const int l = threadIdx.x & 63, r = l & 15, g = l >> 4;
  const int ct0 = HI ? cs : cs - 3;
  const u32 xa = sm + r * 128 + ((g ^ (r >> 1)) * 16);
  const u32 wa = sm + B_XB + (16 * (g >> 1) + 2 * ((r >> 2) + 4 * (g & 1))) * 432 + ct0 * 32 + (l & 3) * 8;
  constexpr int CS = HI ? 0 : 3;
  f4 acc[7][4];
#pragma unroll
  for (int a = 0; a < 7; ++a)
#pragma unroll
    for (int c = 0; c < 4; ++c) {
      if (c == CS && (a < 4) != (HI == 0)) continue;
      acc[a][c] = (f4){0.f, 0.f, 0.f, 0.f};
      OPQ(acc[a][c]);
    }
  u4 xf[2][7];
  u2 wf[2][4][2];
  const u32 wa3 = (cs == 10 && (l & 2)) ? wa - 16 : wa;
  u32 xs, ws, ws3;
  auto rd = [&](int s, int i) {
    if (i == 0) xf[s][0] = ld128(xs ^ (s * 64));
    else if (i <= 8) wf[s][(i - 1) >> 1][(i - 1) & 1] = ldtr((HI && i > 6 ? ws3 : ws) + s * 13824 + ((i - 1) >> 1) * 32 + ((i - 1) & 1) * 432);
    else xf[s][i - 8] = ld128((xs ^ (s * 64)) + (i - 8) * 2048);
  };
  auto step = [&](int s, bool more) {
    int m = 0;
#pragma unroll
    for (int a = 0; a < 7; ++a)
#pragma unroll
      for (int c = 0; c < 4; ++c) {
        if (c == CS && (a < 4) != (HI == 0)) continue;
        mmx(acc[a][c], cat(wf[s][c][0], wf[s][c][1]), xf[s][a]);
        SB();
        if (more && m < 15) rd(s ^ 1, m);
        ++m;
        SB();
      }
  };
  u32 slot = B_SLOT;
  xs = xa + slot;
  ws = wa + slot;
  ws3 = wa3 + slot;
  OPQ(xs);
  OPQ(ws);
  if (HI) OPQ(ws3);
  bar();
  ST(2);
#pragma unroll
  for (int i = 0; i < 15; ++i) rd(0, i);
  SB();
  auto next = [&]() {
    slot = slot == 2 * B_SLOT ? 0 : slot + B_SLOT;
    xs = xa + slot;
    ws = wa + slot;
    ws3 = wa3 + slot;
    bar();
    SB();
  };
#pragma unroll 1
  for (int jj = 0; jj < 7; ++jj) {
    step(0, true);
    next();
    step(1, true);
  }
  step(0, true);
  u32 ia = sm + B_IMG + (2 * ct0 + (g >> 1)) * B_CS + r * 16 + (g & 1) * 8;
  u32 iv = cs == 10 ? ia ^ ((g >> 1) * 128) : ia;
  OPQ(ia);
  OPQ(iv);
  WAITV(0);
  next();
  u4 xl[7];
  u2 wl[4][2], pv;
  constexpr int NT = 25 - HI, LAG = 5;
  auto tile = [](int m) { return HI ? (m < 12 ? m + m / 3 + 1 : m + 4) : (m < 16 ? m : m + (m - 16) / 3); };
  auto rdl = [&](int i) {
    if (i < 8) wl[i >> 1][i & 1] = ldtr((HI && i > 5 ? ws3 : ws) + 13824 + (i >> 1) * 32 + (i & 1) * 432);
    else xl[i - 8] = ld128((xs ^ 64) + (i - 8) * 2048);
  };
#pragma unroll
  for (int m = 0; m < NT; ++m) {
    const int a = tile(m) >> 2, c = tile(m) & 3;
    mmx(acc[a][c], cat(wf[1][c][0], wf[1][c][1]), xf[1][a]);
    SB();
    if (m < 9) rd(0, m);
    else if (m < 18) rdl(m - 9);
    else if (m < 24) rd(0, m - 9);
    SB();
  }
#pragma unroll
  for (int k = 0; k < 2 * NT + LAG + 1; ++k) {
    if (k < 2 * NT) {
      const int a = tile(k >> 1) >> 2, c = tile(k >> 1) & 3;
      if (k & 1) mmx(acc[a][c], cat(wl[c][0], wl[c][1]), xl[a]);
      else mmx(acc[a][c], cat(wf[0][c][0], wf[0][c][1]), xf[0][a]);
    } else if (k == 2 * NT) NOPS(1);
    SB();
    if (k < 6) rdl(9 + k);
    else {
      const int e = k - LAG - 1, a = tile(e >> 1) >> 2, c = tile(e >> 1) & 3;
      if (e & 1) st64(((HI || c >= 2) ? iv : ia) + a * 256 + c * (2 * B_CS), pv);
      else { pv = (u2){pk(acc[a][c][0], acc[a][c][1]), pk(acc[a][c][2], acc[a][c][3])}; OPQ(pv); }
    }
    SB();
  }
}

struct AttnCtx {
  u32 qa, ka, va, va2, pm0, pm1, ob[2], ob4[2];
  f4 b0, b1;
  u64 qm[5];
};
DEV void attn_setup(AttnCtx& x, u32 sm, int w, int par, int sg0, int grp) {
  const int l = threadIdx.x & 63, c = l & 15, g = l >> 4, P = w >> 2, qb = 2 * (w & 3);
  const u32 im = sm + B_IMG + qb * 256;
  x.qa = im + (4 * P + g) * B_CS + (8 + c) * 16;
  x.ka = im + (9 + 4 * P + g) * B_CS + c * 16;
  auto vad = [&](int q) { return im + (18 + (q >> 1)) * B_CS + ((4 * g + (c >> 2)) ^ ((q & 2) * 4)) * 16 + (q & 1) * 8; };
  x.va = vad(9 * P + (l & 3));
  x.va2 = vad(9 * P + 8);
#pragma unroll
  for (int e = 0; e < 4; ++e) {
    x.b0[e] = __uint_as_float((u32)(4 * g + e - c - 1) < 8u ? 0u : 0xff800000u);
    x.b1[e] = __uint_as_float((u32)(4 * g + e - c + 15) < 8u ? 0u : 0xff800000u);
  }
  x.pm0 = (x.b0[0] == 0.f ? 0xffffu : 0u) | (x.b0[1] == 0.f ? 0xffff0000u : 0u);
  x.pm1 = (x.b0[2] == 0.f ? 0xffffu : 0u) | (x.b0[3] == 0.f ? 0xffff0000u : 0u);
#pragma unroll
  for (int k = 0; k < 5; ++k) {
    x.qm[k] = __builtin_amdgcn_ballot_w64((u32)(4 * g - 2 * P + k - 1) < 9u);
    asm volatile("" : "+s"(x.qm[k]));
  }
#pragma unroll
  for (int k = 0; k < 2; ++k) {
    const int qi = 16 * (qb + k) + c, rho = par * 1024 + sg0 + qi;
    const u32 ob = WS_OF + (rho >> 4) * (18 * 1024) + P * 9216;
    const bool ok = qi < 104 && sg0 + qi < 1024;
    x.ob[k] = ok ? ob + grp * 1024 + g * 256 + (rho & 15) * 16 : OOB;
    x.ob4[k] = (ok && g == 0) ? ob + 8192 + grp * 128 + (rho & 15) * 8 : OOB;
  }
  asm volatile("" : "+v"(x.qa), "+v"(x.ka), "+v"(x.va), "+v"(x.va2), "+v"(x.pm0), "+v"(x.pm1), "+v"(x.ob[0]), "+v"(x.ob[1]),
               "+v"(x.ob4[0]), "+v"(x.ob4[1]), "+v"(x.b0), "+v"(x.b1));
}

#define OLD(a, b, s) "buffer_load_dwordx4 " a ", %[p0], %[rs], " s " offen sc1\n\tbuffer_load_dwordx4 " b ", %[p1], %[rs], " s " offen sc1\n\t"
constexpr int UPF = 5;
#define OLD0 "buffer_load_dwordx2 v[244:245], %[q0], %[rs], 0 offen sc1\n\tbuffer_load_dwordx2 v[246:247], %[q0], %[rs], 0 offen offset:128 sc1\n\t" \
             "buffer_load_dwordx2 v[248:249], %[q1], %[rs], 0 offen sc1\n\tbuffer_load_dwordx2 v[250:251], %[q1], %[rs], 0 offen offset:128 sc1\n\t"
#define OLDR OLD("%2", "%3", "%[s1]") OLD("%4", "%5", "%[s2]") OLD("%6", "%7", "%[s3]") OLD("%8", "%9", "%[s4]")
#define OLDI [p0] "v"(p0), [p1] "v"(p1), [q0] "v"(q0), [q1] "v"(q1), [rs] "s"(rs), [s1] "s"(so[1]), [s2] "s"(so[2]), [s3] "s"(so[3]), [s4] "s"(so[4])
#define MARKED "v_max3_u32 %[mx], v244, v245, v246\n\tv_max3_u32 %[mx], %[mx], v247, v248\n\tv_max3_u32 %[mx], %[mx], v249, v250\n\t" \
               "v_max_u32 %[mx], %[mx], v251\n\tv_cmp_eq_u32 vcc, -1, %[mx]\n\t"
DEV void first_issue(u4 (&o)[9][2], u32 q0, u32 q1, u4 rs) {
  asm volatile(OLD0 : "=&{v[244:247]}"(o[0][0]), "=&{v[248:251]}"(o[0][1]) : [q0] "v"(q0), [q1] "v"(q1), [rs] "s"(rs) : "memory");
}
DEV bool first_check(u4 (&o)[9][2], u32& mx, u32 p0, u32 p1, u32 q0, u32 q1, u4 rs, const u32 (&so)[9]) {
  u32 it;
  asm volatile("s_mov_b32 %[it], %[cap]\n\ts_waitcnt vmcnt(8)\n\t" MARKED "s_cbranch_vccz .Lok%=\n"
               ".Lagain%=:\n\ts_sleep 2\n\t" OLD0 "s_waitcnt vmcnt(0)\n\t" MARKED
               "s_cbranch_vccz .Lgot%=\n\ts_sub_u32 %[it], %[it], 1\n\ts_cbranch_scc0 .Lagain%=\n"
               ".Lgot%=:\n\t" NRETRY OLDR ".Lok%=:"
               : "+{v[244:247]}"(o[0][0]), "+{v[248:251]}"(o[0][1]), "+v"(o[1][0]), "+v"(o[1][1]), "+v"(o[2][0]),
                 "+v"(o[2][1]), "+v"(o[3][0]), "+v"(o[3][1]), "+v"(o[4][0]), "+v"(o[4][1]), [mx] "=&v"(mx), [it] "=&s"(it)
               : OLDI, [cap] "n"(SPIN_CAP)
               : "memory", "scc", "vcc");
  return it == 0xffffffffu;
}
struct ProjCtx {
  u32 ob, wb, tw, tr, oo, fo;
};
DEV void proj_setup(ProjCtx& x, u32 sm, int w, int rt2, int cg) {
  const int l = threadIdx.x & 63, c = l & 15, g = l >> 4, rp = w & 3, up = w >> 2;
  x.ob = WS_OF + (8 * rt2 + 2 * rp) * (18 * 1024) + up * 9216 + l * 16;
  x.wb = sm + B_WO + (1 - up) * 36864 + (16 * (g >> 1) + 4 * (g & 1) + (c >> 2)) * 128 + (l & 3) * 8 + (2 * (g & 1) + (c >> 3)) * 32;
  x.tw = sm + B_OT + rp * 4608 + c * 144 + g * 8;
  x.tr = sm + B_OT + rp * 4608 + (l >> 3) * 144 + (l & 7) * 16;
  const int rho = (8 * rt2 + 2 * rp) * 16 + (l >> 3), r0 = (rt2 & 7) * 128;
  x.oo = (2 * (rho & 1023) + (rho >> 10)) * (D_ * 4) + D_ * 2 + cg * 128 + (l & 7) * 16;
  x.fo = w == 3 ? WS_FLAG + (10 * (rt2 >> 3) + min(r0 + 64 * g, r0 + 127) / 104) * 256 + c * 16 : OOB;
  asm volatile("" : "+v"(x.ob), "+v"(x.wb), "+v"(x.tw), "+v"(x.tr), "+v"(x.oo), "+v"(x.fo));
}

DEV void role_b(u32 sm, u32 xcd, u32 i) {
  const int t = threadIdx.x, l = t & 63, w = uni(t >> 6);
  const u32 tl = (xcd < 2 ? 0 : 21 * (xcd >> 1) - 4) + i, rt = tl >> 2;
  const int grp = 4 * (xcd & 1) + (tl & 3), par = rt >= 10, sg0 = 104 * (rt - 10 * par);
  bool has2;
  int rt2, cg;
  auto tile2 = [&]() {
    u32 x = xcd;
    asm volatile("" : "+s"(x));
    has2 = i < (x < 2 ? 17 : (x < 4 ? 19 : 18));
    const u32 t2 = 18 * x - (x < 4 ? (x == 2 ? 2 : x & 1) : 0) + i;
    rt2 = (t2 * 57) >> 9;
    cg = t2 - 9 * rt2;
  };
  AttnCtx ax;
  ProjCtx px;
  u32 vo0 = 0, vo1 = 0;
  auto wo_piece = [&](u4 rwo, int P, int e) {
    const int tt = w & 1, gh = (w >> 1) & 1, n = e < 8 ? 1 + ((e - cg) & 7) : 0;
    dma16<0>(rwo, e < 8 ? vo0 : vo1, ((e < 8 ? 72 * e + 16 * tt + 8 * gh : 72 * (4 * gh + tt) + 32) + 36 * P) * 1152,
                 sm + B_WO + (36 * (1 - P) + 4 * n + (w & 3)) * 1024);
  };
  u2 e2;
  if (__builtin_expect(w >= 4, 1)) {
    const int lw = w - 4;
    u32 v[11];
    int sq = (rt + (tl & 3)) % G1, cnt = 0, st = kstage(xcd, sq);
    {
      const int row = 8 * lw + (l >> 3), sg = sg0 - 8 + row;
      const u32 sw = ((l ^ (row >> 1)) & 7) * 16, swp = sw + par * (D_ * 2);
#pragma unroll
      for (int u = 0; u < 4; ++u) v[u] = __umul24(min(sg + 32 * u, 1023), D_ * 4) + swp;
      v[0] = sg < 0 ? sw : v[0];
    }
    u32 n0 = 64 * lw + l, p9 = __umul24(n0, 7282), p27 = __umul24(n0, 2428), n16 = n0 * 16;
    OPQ(p9); OPQ(p27); OPQ(n16);
    auto woff = [&](int u) {
      const int dn = 256 * u - 896, d = __builtin_amdgcn_sbfe(0xd1e2f30, ((p27 + dn * 2428) >> 14) & 28, 4);
      return __mul24(d, 3456) + (__umul24((p9 + dn * 7282) >> 16, 1152 - 144) + (n16 + dn * 16));
    };
    u32 w3 = woff(3);
    asm volatile("" : "+v"(w3), "+v"(v[3]));
    v[3] = lw < 2 ? v[3] : w3;
#pragma unroll
    for (int u = 0; u < 4; ++u) OPQ(v[u]);
    u32 so0 = st * 128, d0 = sm + B_SLOT + lw * 1024;
    asm volatile("" : "+s"(st), "+s"(so0), "+s"(d0));
    SB();
    WAITL();
    const u4 rx = mk_rs(karg<KX>(), TBYTES);
    dma16<0>(rx, v[0], so0, d0);
    SB();
    const u4 rw = mk_rs(karg<KW>() + grp * 144, 3 * D_ * D_ * 2 - grp * 144);
    const u4 r3 = lw < 2 ? rx : rw;
    const u32 m3 = lw < 2 ? 128 : 64 * 3456;
    auto piece = [&](int u, int stage, u32 slot) {
      const u32 d = sm + slot + (lw + 4 * u) * 1024;
      if (u < 3) dma16<0>(rx, v[u], stage * 128, d);
      else if (u == 3) dma16<0>(r3, v[u], stage * m3, d);
      else if (u < 10 || lw == 0) dma16<0>(rw, v[u], stage * (64 * 3456), d);
    };
#pragma unroll
    for (int u = 1; u < 11; ++u) {
      piece(u, st, B_SLOT);
      if (u < 8) v[3 + u] = woff(3 + u);
      SB();
    }
    auto nxt = [&]() {
      ++cnt;
      sq = cnt >= G1 ? cnt : (sq + 1 == G1 ? 0 : sq + 1);
      st = kstage(xcd, sq);
    };
    tile2();
    attn_setup(ax, sm, w, par, sg0, grp);
    proj_setup(px, sm, w, rt2, cg);
    const u32 wsw = cg * 128 + (((l & 7) ^ (((l >> 4) & 3) << 1)) * 16);
    vo0 = (l >> 3) * 1152 + wsw;
    vo1 = (144 * (l >> 5) + ((l >> 3) & 3)) * 1152 + wsw;
    OPQ(vo0);
    OPQ(vo1);
    constexpr int PRE = 10, WO1 = 7;
    u32 slot = 2 * B_SLOT;
#pragma unroll 1
    for (int jj = 1; jj < 9; ++jj) {
      nxt();
#pragma unroll
      for (int u = 0; u < PRE; ++u) piece(u, st, slot);
      asm volatile("s_waitcnt vmcnt(%0)\n\ts_barrier" ::"n"(PRE) : "memory");
      ST(10 + jj);
#pragma unroll
      for (int u = PRE; u < 11; ++u) piece(u, st, slot);
      slot = slot == 2 * B_SLOT ? 0 : slot + B_SLOT;
    }
    const u4 rwo = mk_rs(karg<KWO>(), has2 ? D_ * D_ * 2 : 0);
    const auto bw = mk_br(karg<KWS>(), WS_USED);
    auto mark = [&]() {
      asm volatile("buffer_load_dwordx2 %0, %1, %2, 0 offen sc1" : "=&v"(e2) : "v"(WS_EPOCH), "s"(mk_rs(karg<KWS>(), WS_USED)) : "memory");
#pragma unroll
      for (int h = 0; h < 4; ++h) {
        __builtin_amdgcn_raw_buffer_store_b128((u4){~0u, ~0u, ~0u, ~0u}, bw, ax.ob[h & 1] - (h >> 1) * 9216, 0, 16);
        __builtin_amdgcn_raw_buffer_store_b64((u2){~0u, ~0u}, bw, ax.ob4[h & 1] - (h >> 1) * 9216, 0, 16);
      }
    };
#pragma unroll
    for (int e = 0; e < WO1; ++e) wo_piece(rwo, 0, e);
    asm volatile("s_waitcnt vmcnt(%0)\n\ts_barrier" ::"n"(WO1) : "memory");
    ST(19);
#pragma unroll
    for (int e = WO1; e < 9; ++e) wo_piece(rwo, 0, e);
    mark();
    asm volatile("s_waitcnt vmcnt(0)" : "+v"(e2)::"memory");
    e2 = tag_next(e2);
    __builtin_amdgcn_raw_buffer_store_b64(e2, bw, l ? OOB : WS_FLAG + rt * 256 + grp * 32 + lw * 8, 0, 16);
    ST(3);
    bar();
#pragma unroll
    for (int e = 0; e < 9; ++e) wo_piece(rwo, 1, e);
  } else {
    tile2();
    attn_setup(ax, sm, w, par, sg0, grp);
    proj_setup(px, sm, w, rt2, cg);
    WAITL();
    const auto bw = mk_br(karg<KWS>(), WS_USED);
    e2 = __builtin_amdgcn_raw_buffer_load_b64(bw, w == 3 ? WS_EPOCH : OOB, 0, 16);
    OPQ(e2);
    WAITV(0);
    __builtin_amdgcn_raw_buffer_store_b64(e2, bw, (w == 3 && l < 4) ? WS_FLAG + rt * 256 + grp * 32 + l * 8 : OOB, 0, 16);
    e2 = tag_next(e2);
    OPQ(e2);
    __builtin_amdgcn_s_setprio(1);
    if (w & 1) qkv_gemm<1>(sm, w < 2 ? 3 : 10);
    else qkv_gemm<0>(sm, w < 2 ? 3 : 10);
    ST(3);
    bar();
  }
  ST(4);
  const int g = l >> 4, up = w >> 2;
  u4 fl;
  {
    const float scale = 0.23570226039551584f, cs = scale * 1.4426950408889634f;
    const u32 pm0 = ax.pm0, pm1 = ax.pm1;
    u4 qf[4], kf[2][3], vf[2][3];
#pragma unroll
    for (int hh = 0; hh < 4; ++hh) {
      qf[hh] = ld128(ax.qa + (hh & 1) * (2 * B_CS) + (hh >> 1) * 256);
      if (hh < 2) kf[hh][0] = ld128(ax.ka + hh * (2 * B_CS));
      kf[hh & 1][(hh >> 1) + 1] = ld128(ax.ka + (hh & 1) * (2 * B_CS) + ((hh >> 1) + 1) * 256);
    }
    SB();
    u4 ones = {0x3f803f80u, 0x3f803f80u, 0x3f803f80u, 0x3f803f80u};
    OPQ(ones);
    const u4 rws = mk_rs(karg<KWS>(), WS_USED);
    const auto bw = mk_br(karg<KWS>(), WS_USED);
    SB();
    f4 s0[4], s1[4];
    float red[4];
    auto mask = [&](int hh, int d) { qf[hh][d] = __builtin_amdgcn_inverse_ballot_w64(ax.qm[d - (hh & 1) + 1]) ? qf[hh][d] : 0u; };
#pragma unroll
    for (int i = 0; i < 6; ++i) mask(i >> 2, i & 3);
    SB();
#pragma unroll
    for (int m = 0; m < 8; ++m) {
      const int hh = m >> 1;
      if (m & 1) s1[hh] = mma(kf[hh & 1][(hh >> 1) + 1], qf[hh], ax.b1);
      else s0[hh] = mma(kf[hh & 1][hh >> 1], qf[hh], ax.b0);
      SB();
      if (m < 5) { mask((6 + 2 * m) >> 2, (6 + 2 * m) & 3); mask((7 + 2 * m) >> 2, (7 + 2 * m) & 3); }
      SB();
    }
    u2 vh[12];
#pragma unroll
    for (int i = 0; i < 12; ++i) {
      const int k = i / 6, ct = (i % 6) >> 1;
      vh[i] = ldtr((ct < 2 ? ax.va + ct * (2 * B_CS) : ax.va2) + (k + (i & 1)) * 256);
      asm volatile("" ::: "memory");
    }
#pragma unroll
    for (int i = 0; i < 6; ++i) vf[i / 3][i % 3] = cat(vh[2 * i], vh[2 * i + 1]);
    if (sg0 == 0 && (w & 3) == 0) {
#pragma unroll
      for (int hh = 0; hh < 2; ++hh)
#pragma unroll
        for (int e = 0; e < 4; ++e) s0[hh][e] = g < 2 ? ax.b0[e] - 1e9f / scale : s0[hh][e];
    }
#pragma unroll
    for (int hh = 0; hh < 4; ++hh) {
#pragma unroll
      for (int e = 0; e < 4; ++e) s0[hh][e] = fmx(s0[hh][e], s1[hh][e]);
      red[hh] = fmx(fmx(s0[hh][0], s0[hh][1]), fmx(s0[hh][2], s0[hh][3]));
    }
    rows_spread(-cs * rows_max(red), red);
    SB();
    f4 oa[4], ob[4], sum[4];
    u4 pf[4], o;
    float ia, ib, im;
    auto ex = [&](int hh, int i) {
      if (i < 2) {
#pragma unroll
        for (int e = 2 * i; e < 2 * i + 2; ++e) s0[hh][e] = __builtin_amdgcn_exp2f(__builtin_fmaf(s0[hh][e], cs, red[hh]));
      } else {
        const u32 p0 = pk(s0[hh][0], s0[hh][1]), p1 = pk(s0[hh][2], s0[hh][3]);
        pf[hh] = (u4){p0 & pm0, p1 & pm1, p0 & ~pm0, p1 & ~pm1};
      }
      SB();
    };
    auto pv = [&](int hh, int i) {
      if (i == 0) mm0(sum[hh], ones, pf[hh]);
      if (i == 1) mm0(oa[hh], vf[hh >> 1][hh & 1], pf[hh]);
      if (i == 2) mm0(ob[hh], vf[hh >> 1][(hh & 1) + 1], pf[hh]);
      SB();
    };
    auto out = [&](int k, int i) {
      const f4 t0 = oa[2 * k], t1 = oa[2 * k + 1], tp = ob[2 * k], t2 = ob[2 * k + 1];
      if (i == 0) {
        ia = __builtin_amdgcn_rcpf(sum[2 * k][0]);
        o.x = pm(t0[0] * ia, t0[1] * ia);
        o.y = pm(t0[2] * ia, t0[3] * ia);
      } else if (i == 1) {
        ib = __builtin_amdgcn_rcpf(sum[2 * k + 1][0]);
        im = g == 0 ? ia : ib;
        o.z = pm((g == 0 ? tp[0] : t1[0]) * im, (g == 0 ? tp[1] : t1[1]) * im);
        o.w = pm(t1[2] * ib, t1[3] * ib);
        __builtin_amdgcn_raw_buffer_store_b128(o, bw, ax.ob[k], 0, 16);
      } else {
        __builtin_amdgcn_raw_buffer_store_b64((u2){pm(t2[0] * ib, t2[1] * ib), pm(t2[2] * ib, t2[3] * ib)}, bw, ax.ob4[k], 0, 16);
      }
      SB();
    };
    ex(0, 0); ex(0, 1); ex(0, 2);
#pragma unroll
    for (int hh = 1; hh < 4; ++hh) { ex(hh, 0); pv(hh - 1, 0); ex(hh, 1); pv(hh - 1, 1); ex(hh, 2); pv(hh - 1, 2); }
    asm volatile("buffer_load_dwordx4 %0, %1, %2, 0 offen sc1" : "=&v"(fl) : "v"(px.fo), "s"(rws) : "memory");
    SB();
    pv(3, 0); out(0, 0); pv(3, 1); out(0, 1); pv(3, 2); out(0, 2);
    out(1, 0); out(1, 1); out(1, 2);
#pragma unroll
    for (int hh = 0; hh < 4; ++hh) asm volatile("" ::"v"(sum[hh]), "v"(ob[hh]));
  }
  ST(5);
  if (has2) {
    asm volatile("s_cmp_eq_u32 %0, 0\n\ts_cbranch_scc1 .Lnb%=\n\ts_waitcnt vmcnt(5)\n\ts_barrier\n.Lnb%=:" ::"s"(up) : "memory", "scc");
    SB();
    const u4 rws = mk_rs(karg<KWS>(), WS_USED);
    const u32 ob0 = px.ob, ob1 = ob0 + 18 * 1024;
    u4 of[9][2];
    u32 so[9];
#pragma unroll
    for (int n = 0; n < 9; ++n) so[n] = n ? ((n - 1 + cg) & 7) * 1024 : 8192;
    u32 mx;
    auto oload = [&](int n) {
      asm volatile(OLD("%0", "%1", "%[s]") : "=&v"(of[n][0]), "=&v"(of[n][1]) : [p0] "v"(ob0), [p1] "v"(ob1), [rs] "s"(rws), [s] "s"(so[n]) : "memory");
    };
    auto arrive = [&](int n) {
      asm volatile("s_waitcnt vmcnt(%2)" : "+v"(of[n][0]), "+v"(of[n][1]) : "n"(2 * (n + UPF - 1 < 8 ? UPF - 1 : 8 - n)) : "memory");
    };
    auto scan = [&](int n, int i) { mx = max(max(mx, of[n][i >> 1][2 * (i & 1)]), of[n][i >> 1][2 * (i & 1) + 1]); };
    f4 acc[2][4];
    constexpr int NP1 = 4, NP2 = 7, NPRE = 8;
    u2 wv[9][4][2];
    auto pre = [&](int n0, int n1) {
#pragma unroll
      for (int n = n0; n < n1; ++n) {
#pragma unroll
        for (int ct = 0; ct < 4; ++ct) {
          wv[n][ct][0] = ldtr((px.wb ^ (ct * 32)) + n * 4096);
          wv[n][ct][1] = ldtr((px.wb ^ (ct * 32)) + n * 4096 + 1024);
        }
        SB();
      }
    };
    const u32 q0 = ob0 + 8192 - (l & 15) * 8, q1 = q0 + 18 * 1024;
    _Pragma("unroll 1") for (int pass = 0; pass < SPIN_CAP; ++pass) {
      pre(0, NP1);
      asm volatile("s_waitcnt vmcnt(4)" : "+v"(fl)::"memory");
      if (__builtin_expect(w == 3 && pass == 0 && !tag_all(fl, e2), 0)) {
        _Pragma("unroll 1") for (int it = 0; it < SPIN_CAP; ++it) {
          asm volatile("" ::: "memory");
          if (tag_all(__builtin_amdgcn_raw_buffer_load_b128(mk_br(karg<KWS>(), WS_USED), px.fo, 0, 16), e2)) break;
          __builtin_amdgcn_s_sleep(4);
        }
      }
      if ((pass | up) == 0) asm volatile("s_barrier" ::: "memory");
      ST(6);
      first_issue(of, q0, q1, rws);
      pre(NP1, NP2);
#pragma unroll
      for (int n = 1; n < UPF; ++n) oload(n);
      pre(NP2, NPRE);
      const bool quit = first_check(of, mx, ob0, ob1, q0, q1, rws, so);
      ST(7);
#pragma unroll
      for (int n = 0; n < 7; ++n) {
#pragma unroll
        for (int m = 0; m < 8; ++m) {
          const u4 wf = cat(wv[n][m >> 1][0], wv[n][m >> 1][1]);
          if (n) mmx(acc[m & 1][m >> 1], wf, of[n][m & 1]);
          else mm0(acc[m & 1][m >> 1], wf, of[n][m & 1]);
          SB();
          if (m == 0 && n + UPF < 9) oload(n + UPF);
          if (n + NPRE < 9) wv[n + NPRE][m >> 1][m & 1] = ldtr((px.wb ^ ((m >> 1) * 32)) + (n + NPRE) * 4096 + (m & 1) * 1024);
          if (n && m >= 2 && m < 6) { scan(n, m - 2); OPQ(mx); }
          if (m == 7) arrive(n + 1);
          SB();
        }
      }
      arrive(8);
#pragma unroll
      for (int i = 0; i < 8; ++i) scan(7 + (i >> 2), i & 3);
      if (__builtin_expect(!__any(mx == 0xffffffffu) || quit, 1)) break;
    }
    const u32 xb = sm + B_RED + (w & 3) * 8192 + l * 16;
    auto last = [&](int n, auto&& gap) {
#pragma unroll
      for (int m = 0; m < 8; ++m) {
        acc[m & 1][m >> 1] = mma(cat(wv[n][m >> 1][0], wv[n][m >> 1][1]), of[n][m & 1], acc[m & 1][m >> 1]);
        SB();
        gap(m);
        SB();
      }
    };
    if (!up) {
      last(7, [](int) {});
      last(8, [](int) {});
      ST(8);
#pragma unroll
      for (int m = 0; m < 8; ++m) st128(xb + m * 1024, __builtin_bit_cast(u4, acc[m & 1][m >> 1]));
      bar();
    } else {
      const auto bo = mk_br(karg<KOUT>(), 2 * TBYTES);
      bar();
      f4 part[8];
      last(7, [&](int m) { part[m] = __builtin_bit_cast(f4, ld128(xb + m * 1024)); });
      acc[0][0] += part[0];
      acc[1][0] += part[1];
      SB();
      last(8, [&](int m) { if (m < 6) acc[m & 1][(m >> 1) + 1] += part[m + 2]; });
      ST(8);
#pragma unroll
      for (int m = 0; m < 8; ++m) {
        const f4 r = acc[m & 1][m >> 1];
        st64(px.tw + (m & 1) * 2304 + (m >> 1) * 32, (u2){pk(r[0], r[1]), pk(r[2], r[3])});
      }
      u4 o[4];
#pragma unroll
      for (int hf = 0; hf < 4; ++hf) o[hf] = ld128(px.tr + hf * 1152);
#pragma unroll
      for (int hf = 0; hf < 4; ++hf) __builtin_amdgcn_raw_buffer_store_b128(o[hf], bo, px.oo + hf * (16 * D_ * 4), 0, 16);
    }
    ST(9);
  } else {
    asm volatile("s_waitcnt vmcnt(0)" ::"v"(fl) : "memory");
  }
}

DEV u32 warm_row(u32 xcd, u32 r) {
  const u32 rt0 = xcd < 2 ? 0 : (21 * (xcd >> 1) - 4) >> 2, q = rt0 * 104 + r - 8, sg = q >= 1040 ? q - 1040 : q;
  return (r < (xcd < 2 ? 528 : 632) && sg < 1024) ? sg * (D_ * 4) + (q >= 1040 ? D_ * 2 : 0) : OOB;
}
DEV void role_s(u32 sm, u32 xcd, u32 k3) {
  const u32 t = threadIdx.x, l = t & 63, sw = k3 * 8 + uni(t >> 6);
  const u32 k5 = (sw * 13) >> 6;
  u32 vw = l * 3456 + k5 * 1152 + ((xcd & 1) * 4 + sw - 5 * k5) * 128, vx = warm_row(xcd, (sw - 15) * 64 + l);
  OPQ(vw);
  OPQ(vx);
  const u32 vt = sw < 15 ? vw : vx, mt = sw < 15 ? 64 * 3456 : 128;
  WAITL();
  const u4 rw = mk_rs(karg<KW>(), 3 * D_ * D_ * 2), rx = mk_rs(karg<KX>(), TBYTES), rt = sw < 15 ? rw : rx;
  touch_lines(rt, vt, kstage(xcd, G1) * mt, sm);
  bar();
  {
    const u32 m = (sw % 8) * 3 + k3, h = m >> 1, a = xcd >> 1, rt0 = xcd < 2 ? 0 : (21 * a - 4) >> 2;
    const u32 r = h < a ? 0 : (xcd < 2 ? 4 : 5), g = h < a ? h : h + 1;
    if (m < 6) touch_lines(rx, warm_row(xcd, r * 104 + (m & 1) * 56 + min(l, 55u)), kstage(xcd, (rt0 + r + g) % G1) * 128, sm);
    else if (m < 15 - G1) touch_lines(rx, warm_row(xcd, 576 + l), kstage(xcd, G1 + m - 6) * 128, sm);
    bar();
  }
  ST(3);
#pragma unroll 1
  for (u32 st = G1 + 1; st < 9; ++st) {
    touch_lines(rt, vt, kstage(xcd, st) * mt, sm);
    bar();
    ST(st);
  }
  if (sw & 1) touch_lines(mk_rs(karg<KWO>(), D_ * D_ * 2), min(l, 53u) * 128, (xcd * 12 + (sw >> 1)) * (54 * 128), sm);
  {
    const u4 rq = mk_rs(karg<KQ>(), TBYTES);
    const u32 m = (sw % 8) * 3 + k3;
    bar();
    if (m < 16 || xcd < 2) touch_lines(rq, l * (D_ * 2), (m < 16 ? 16 + m : m) * 64 * (D_ * 2) + (m < 16 ? xcd : 8) * 128, sm);
    if (xcd < 2 && m < 8) touch_lines(rq, l * (D_ * 2), (24 + m) * 64 * (D_ * 2) + 8 * 128, sm);
  }
  WAITV(0);
  ST(2);
  if (xcd == 0 && sw == 0) {
    const auto bw = mk_br(karg<KWS>(), WS_USED);
    const u2 ev = tag_next(__builtin_amdgcn_raw_buffer_load_b64(bw, WS_EPOCH, 0, 16));
    _Pragma("unroll 1") for (int it = 0; it < SPIN_CAP; ++it) {
      __builtin_amdgcn_s_sleep(16);
      asm volatile("" ::: "memory");
      u4 d = {0u, 0u, 0u, 0u};
#pragma unroll
      for (int k = 0; k < 5; ++k) d |= __builtin_amdgcn_raw_buffer_load_b128(bw, WS_FLAG + k * 1024 + l * 16, 0, 16) ^ cat(ev, ev);
      if (__all((d.x | d.y | d.z | d.w) == 0)) break;
    }
    if (l == 0) __builtin_amdgcn_raw_buffer_store_b64(ev, bw, WS_EPOCH, 0, 16);
  }
}

#if defined(__HIP_DEVICE_COMPILE__)
__attribute__((target("no-packed-fp32-ops")))
#endif
__global__ __launch_bounds__(512, 2) void fused_kernel(Params p) {
  __shared__ __attribute__((aligned(1024))) char smem[LDS_BYTES];
  ST_BEGIN;
  asm volatile("s_load_dwordx16 s[76:91], %0, 0x0\n\ts_load_dwordx4 s[92:95], %0, 0x40\n\ts_load_dwordx2 s[96:97], %0, 0x50"
               ::"s"(__builtin_amdgcn_kernarg_segment_ptr())
               : "s76", "s77", "s78", "s79", "s80", "s81", "s82", "s83", "s84", "s85", "s86", "s87", "s88", "s89", "s90", "s91", "s92", "s93", "s94", "s95", "s96", "s97");
  const u32 sm = (u32)(size_t)(LAS char*)smem;
  const u32 b = blockIdx.x, xcd = b & 7, i = ((b >> 3) - 3) & 31;
  {
    u64 pc;
    asm volatile("s_getpc_b64 %0" : "=s"(pc));
    const u4 rc = mk_rs((const void*)(pc & ~255ull), CODE_BYTES);
    const u32 off = threadIdx.x < 10 ? (i + 32 * threadIdx.x) * 128 : OOB;
    asm volatile("s_mov_b32 m0, %2\n\ts_nop 0\n\tbuffer_load_dword %0, %1, 0 offen lds" ::"v"(off), "s"(rc), "s"(sm + LDS_DUMMY) : "memory");
  }
  const u32 nb = xcd < 2 ? 17 : 21;
  if (__builtin_expect(i >= 29, 0)) role_s(sm, xcd, i - 29);
  else if (__builtin_expect(i >= nb, 0)) role_a(sm, i >= 21 ? 8 * xcd + i - 21 : 64 + 4 * xcd + i - 17);
  else role_b(sm, xcd, i);
  ST_END;
}

extern "C" void kernel_launch(void* const* d_in, const int* in_sizes, int n_in, void* d_out, int out_size,
                              void* d_ws, size_t ws_size, hipStream_t stream) {
  Params p;
  p.q = (const u16*)d_in[0];
  p.k = (const u16*)d_in[1];
  p.v = (const u16*)d_in[2];
  p.sk = (const u16*)d_in[3];
  p.h = (const u16*)d_in[4];
  p.dd = (const u16*)d_in[5];
  p.x = (const u16*)d_in[6];
  p.w = (const u16*)d_in[7];
  p.wo = (const u16*)d_in[8];
  p.out = (u16*)d_out;
  p.ws = (char*)d_ws;
  fused_kernel<<<256, 512, 0, stream>>>(p);
}
```
